```python
import torch

def _softmax(x, axis=-1):
    return torch.softmax(x, dim=axis)
def _gelu(x):
    return torch.nn.functional.gelu(x, approximate="tanh")
def _silu(x):
    return torch.nn.functional.silu(x)
def _sigmoid(x):
    return torch.sigmoid(x)
def _layernorm(x, axis=-1, eps=1e-5):
    m = x.mean(dim=axis, keepdim=True)
    v = x.var(dim=axis, keepdim=True, unbiased=False)
    return (x - m) / torch.sqrt(v + eps)
def _scatter_add(tgt, idx, src, axis=0):
    return tgt.clone().index_add_(axis, idx, src)
def _segment_sum(src, seg, n):
    out = torch.zeros((n,) + src.shape[1:], dtype=src.dtype)
    return out.index_add_(0, seg, src)
def _pad(x, pw):
    flat = tuple(v for pair in reversed(pw) for v in pair)
    return torch.nn.functional.pad(x, flat)

kernel_name = 'concat_heads__mha_block_cached__mha_block_cached__near_ridge'

def setup_inputs():

    _w = (lambda a: (lambda t: t.to(torch.bfloat16) if t.is_floating_point() else t)(torch.as_tensor(a)))
    def _A_setup():
        B, Sq, D = 16, 96, 576
        H, Sk, Dh = 32, 512, 128
        return {
            "x": _w((lambda *s: torch.randn(*s, dtype=torch.bfloat16) * 0.8)(B, Sq, D)),
            "k_cache": _w((lambda *s: torch.randn(*s, dtype=torch.bfloat16) * 0.8)(B, H, Sk, Dh)),
            "v_cache": _w((lambda *s: torch.randn(*s, dtype=torch.bfloat16) * 0.05)(B, H, Sk, Dh)),
            "wq": _w((lambda *s: torch.randn(*s, dtype=torch.bfloat16) * 0.8)(D, H * Dh) * (D ** -0.5)),
            "wo": _w((lambda *s: torch.randn(*s, dtype=torch.bfloat16) * 0.05)(H * Dh, D) * ((H * Dh) ** -0.5)),
        }
    _A_in = _A_setup()

    def _B_setup():
        B, Sq, D = 16, 96, 576
        H, Sk, Dh = 32, 512, 128
        return {
            "x": _w((lambda *s: torch.randn(*s, dtype=torch.bfloat16) * 0.8)(B, Sq, D)),
            "k_cache": _w((lambda *s: torch.randn(*s, dtype=torch.bfloat16) * 0.8)(B, H, Sk, Dh)),
            "v_cache": _w((lambda *s: torch.randn(*s, dtype=torch.bfloat16) * 0.05)(B, H, Sk, Dh)),
            "wq": _w((lambda *s: torch.randn(*s, dtype=torch.bfloat16) * 0.8)(D, H * Dh) * (D ** -0.5)),
            "wo": _w((lambda *s: torch.randn(*s, dtype=torch.bfloat16) * 0.05)(H * Dh, D) * ((H * Dh) ** -0.5)),
        }
    _B_in = _B_setup()

    return {
        "A_x": _A_in["x"],
        "A_k_cache": _A_in["k_cache"],
        "A_v_cache": _A_in["v_cache"],
        "A_wq": _A_in["wq"],
        "A_wo": _A_in["wo"],
        "B_x": _B_in["x"],
        "B_k_cache": _B_in["k_cache"],
        "B_v_cache": _B_in["v_cache"],
        "B_wq": _B_in["wq"],
        "B_wo": _B_in["wo"],
    }

def reference(A_x, A_k_cache, A_v_cache, A_wq, A_wo, B_x, B_k_cache, B_v_cache, B_wq, B_wo):
    def _A_ref(x, k_cache, v_cache, wq, wo):
        B, Sq, D = x.shape
        H, Dh = 32, 128
        q = (lambda x, *d: x.permute(*d))((x @ wq).reshape(B, Sq, H, Dh), 0, 2, 1, 3)
        scores = torch.einsum("bhqd,bhkd->bhqk", q, k_cache) * (Dh ** -0.5)
        p = _softmax(scores, axis=-1)
        o = torch.einsum("bhqk,bhkd->bhqd", p, v_cache)
        return (lambda x, *d: x.permute(*d))(o, 0, 2, 1, 3).reshape(B, Sq, H * Dh) @ wo

    def _B_ref(x, k_cache, v_cache, wq, wo):
        B, Sq, D = x.shape
        H, Dh = 32, 128
        q = (lambda x, *d: x.permute(*d))((x @ wq).reshape(B, Sq, H, Dh), 0, 2, 1, 3)
        scores = torch.einsum("bhqd,bhkd->bhqk", q, k_cache) * (Dh ** -0.5)
        p = _softmax(scores, axis=-1)
        o = torch.einsum("bhqk,bhkd->bhqd", p, v_cache)
        return (lambda x, *d: x.permute(*d))(o, 0, 2, 1, 3).reshape(B, Sq, H * Dh) @ wo

    a = _A_ref(A_x, A_k_cache, A_v_cache, A_wq, A_wo)
    b = _B_ref(B_x, B_k_cache, B_v_cache, B_wq, B_wo)
    return torch.concatenate([a, b], axis=-1)
```

```cpp
#include <hip/hip_runtime.h>
#include <hip/hip_bf16.h>
#include <stdint.h>

#define LAS __attribute__((address_space(3)))
#define DI __device__ __forceinline__
typedef __bf16 bf16x2 __attribute__((ext_vector_type(2)));
typedef __bf16 bf16x4 __attribute__((ext_vector_type(4)));
typedef __bf16 bf16x8 __attribute__((ext_vector_type(8)));
typedef float f32x2 __attribute__((ext_vector_type(2)));
typedef float f32x4 __attribute__((ext_vector_type(4)));
typedef float f32x16 __attribute__((ext_vector_type(16)));
typedef unsigned u32x4 __attribute__((ext_vector_type(4)));
typedef unsigned u32x2 __attribute__((ext_vector_type(2)));

constexpr int NB = 16, SQ = 96, DM = 576, NH = 32, SK = 512, DH = 128, HD = NH * DH;
constexpr unsigned SLOT = 16384, NSLOT = 5;
constexpr int EPI = 34;
constexpr int NEL = 2 * EPI;
constexpr float SC_LOG2E = 0.08838834764831845f * 1.4426950408889634f;
constexpr float THR_RAW = 8.0f / SC_LOG2E;

DI unsigned rfl(unsigned x) { return __builtin_amdgcn_readfirstlane(x); }
DI unsigned cvtpk(float lo, float hi) { f32x2 v = {lo, hi}; bf16x2 b = __builtin_convertvector(v, bf16x2); return __builtin_bit_cast(unsigned, b); }
DI bf16x8 pack8(const f32x16& x, int s, float sc) {
  u32x4 w = {cvtpk(x[8 * s + 0] * sc, x[8 * s + 1] * sc), cvtpk(x[8 * s + 2] * sc, x[8 * s + 3] * sc),
             cvtpk(x[8 * s + 4] * sc, x[8 * s + 5] * sc), cvtpk(x[8 * s + 6] * sc, x[8 * s + 7] * sc)};
  return __builtin_bit_cast(bf16x8, w);
}
DI bf16x8 pack8(const f32x16& x, int s) {
  u32x4 w = {cvtpk(x[8 * s + 0], x[8 * s + 1]), cvtpk(x[8 * s + 2], x[8 * s + 3]),
             cvtpk(x[8 * s + 4], x[8 * s + 5]), cvtpk(x[8 * s + 6], x[8 * s + 7])};
  return __builtin_bit_cast(bf16x8, w);
}
DI bf16x8 lds_b128(unsigned a) { return *(const LAS bf16x8*)(uintptr_t)a; }
DI bf16x8 lds_tr2(unsigned a0, unsigned a1) {
  bf16x4 lo = __builtin_amdgcn_ds_read_tr16_b64_v4bf16((LAS bf16x4*)(uintptr_t)a0);
  bf16x4 hi = __builtin_amdgcn_ds_read_tr16_b64_v4bf16((LAS bf16x4*)(uintptr_t)a1);
  return __builtin_shufflevector(lo, hi, 0, 1, 2, 3, 4, 5, 6, 7);
}
DI float xhalf_max(float v) { auto r = __builtin_amdgcn_permlane32_swap(__float_as_uint(v), __float_as_uint(v), false, false); return fmaxf(__uint_as_float(r[0]), __uint_as_float(r[1])); }
DI float xhalf_sum(float v) { auto r = __builtin_amdgcn_permlane32_swap(__float_as_uint(v), __float_as_uint(v), false, false); return __uint_as_float(r[0]) + __uint_as_float(r[1]); }
#define MFMA32(a, b, c) __builtin_amdgcn_mfma_f32_32x32x16_bf16((a), (b), (c), 0, 0, 0)
#define MFMA16(a, b, c) __builtin_amdgcn_mfma_f32_16x16x32_bf16((a), (b), (c), 0, 0, 0)
#define BAR() asm volatile("s_waitcnt lgkmcnt(0)\n\ts_barrier" ::: "memory")
DI __amdgpu_buffer_rsrc_t mkrsrc(const void* p, unsigned bytes) { return __builtin_amdgcn_make_buffer_rsrc((void*)p, (short)0, (int)bytes, 0x00020000); }
template <int AUX> DI void dma16(__amdgpu_buffer_rsrc_t r, unsigned lds, unsigned voff, unsigned soff) {
  __builtin_amdgcn_raw_ptr_buffer_load_lds(r, (LAS void*)(uintptr_t)lds, 16, voff, soff, 0, AUX);
}

struct Ptrs { const void* x[2]; const void* k[2]; const void* v[2]; const void* wq[2]; const void* wo[2]; };

__global__ __launch_bounds__(256, 2) void attn_main(const char* xA, const char* kA, const char* vA, const char* wqA,
                                                    const char* xB, const char* kB, const char* vB, const char* wqB, char* ows) {
  extern __shared__ __attribute__((aligned(16))) char lds[];
  const unsigned ldsb = (unsigned)(uintptr_t)(LAS char*)lds;
  const unsigned wid = rfl(threadIdx.x >> 6), lane = threadIdx.x & 63;
  const unsigned id = blockIdx.x, xcd = id & 7, idx = id >> 3, blk = xcd >> 2, sub = xcd & 3;
  const unsigned b = 8 * (sub & 1) + (idx & 7), h0 = 16 * (sub >> 1) + (idx >> 3);

  if (wid == 3) {
    const char* xp = blk ? xB : xA; const char* kp = blk ? kB : kA; const char* vp = blk ? vB : vA; const char* wp = blk ? wqB : wqA;
    const __amdgpu_buffer_rsrc_t rx = mkrsrc(xp + (size_t)b * SQ * DM * 2, SQ * DM * 2);
    const __amdgpu_buffer_rsrc_t rw = mkrsrc(wp, DM * HD * 2);
    const __amdgpu_buffer_rsrc_t rk = mkrsrc(kp + (size_t)b * NH * SK * DH * 2, NH * SK * DH * 2);
    const __amdgpu_buffer_rsrc_t rv = mkrsrc(vp + (size_t)b * NH * SK * DH * 2, NH * SK * DH * 2);
    const unsigned l4 = lane >> 4, l15 = lane & 15;
    unsigned vk[4], vx[2];
#pragma unroll
    for (int i = 0; i < 4; ++i) vk[i] = l4 * 256 + 16 * (l15 ^ (4 * i + l4));
#pragma unroll
    for (int i = 0; i < 2; ++i) vx[i] = (lane >> 3) * (DM * 2) + 16 * ((lane & 7) ^ ((4 * i + l4) & 7));
    const unsigned vv = l4 * 256 + 16 * (l15 ^ (l4 << 2));
    const unsigned vw = l4 * (HD * 2) + 16 * (l15 ^ (l4 << 2));
    auto issue = [&](int e) {
      const unsigned it = e >= EPI, le = e - it * EPI, h = h0 + 8 * it;
      const unsigned dst = ldsb + (unsigned)(e % NSLOT) * SLOT;
      if (le < 18) {
        const unsigned kc = le >> 1;
        if ((le & 1) == 0) {
#pragma unroll
          for (int p = 0; p < 12; ++p) dma16<0>(rx, dst + p * 1024, vx[p & 1], kc * 128 + p * 8 * (DM * 2));
        } else {
#pragma unroll
          for (int p = 0; p < 16; ++p) dma16<0>(rw, dst + p * 1024, vw, (kc * 64 + p * 4) * (HD * 2) + h * 256);
        }
      } else {
        const unsigned t = (le - 18) >> 1, so = (h * SK + t * 64) * 256;
        if ((le & 1) == 0) {
#pragma unroll
          for (int p = 0; p < 16; ++p) dma16<2>(rk, dst + p * 1024, vk[p & 3], so + p * 1024);
        } else {
#pragma unroll
          for (int p = 0; p < 16; ++p) dma16<2>(rv, dst + p * 1024, vv, so + p * 1024);
        }
      }
    };
    auto pieces = [&](int e) -> int { if (e >= NEL) return 0; const int le = e >= EPI ? e - EPI : e; return (le < 18 && (le & 1) == 0) ? 12 : 16; };
    for (int e = 0; e < 4; ++e) issue(e);
    for (int e = 0; e < NEL; ++e) {
      const int n = pieces(e + 1) + pieces(e + 2) + pieces(e + 3);
      if (n == 48) asm volatile("s_waitcnt vmcnt(48)" ::: "memory");
      else if (n == 44) asm volatile("s_waitcnt vmcnt(44)" ::: "memory");
      else if (n == 40) asm volatile("s_waitcnt vmcnt(40)" ::: "memory");
      else if (n == 32) asm volatile("s_waitcnt vmcnt(32)" ::: "memory");
      else if (n == 16) asm volatile("s_waitcnt vmcnt(16)" ::: "memory");
      else asm volatile("s_waitcnt vmcnt(0)" ::: "memory");
      asm volatile("s_barrier" ::: "memory");
      if (e + 4 < NEL) issue(e + 4);
    }
    return;
  }

  const unsigned r = lane & 31, hh = lane >> 5, q4 = (lane & 15) >> 2, p4 = lane & 3, bk = (lane >> 4) & 1;
  const unsigned xrow = 32 * wid + r;
  const unsigned xoff = xrow * 128 + 16 * (hh ^ ((xrow >> 1) & 7));
  const unsigned woff = 256 * (8 * hh + q4) + 64 * q4 + 32 * bk + 16 * (p4 & 1) + 8 * (p4 >> 1);
  const unsigned koff = 256 * r + 16 * (hh ^ (r & 15));
  const unsigned voff = 256 * (4 * hh + q4) + 64 * q4 + 32 * bk + 8 * p4;
  unsigned sb = ldsb;
  const unsigned send = ldsb + NSLOT * SLOT;
  auto next = [&]() { sb += SLOT; if (sb == send) sb = ldsb; };

  for (int it = 0; it < 2; ++it) {
    const unsigned h = h0 + 8 * it;
    f32x16 acc[4];
#pragma unroll
    for (int d = 0; d < 4; ++d) acc[d] = f32x16{};
    for (int kc = 0; kc < 9; ++kc) {
      BAR();
      bf16x8 xf[4];
#pragma unroll
      for (int s = 0; s < 4; ++s) xf[s] = lds_b128((sb + xoff) ^ (32 * s));
      next();
      BAR();
#pragma unroll
      for (int s = 0; s < 4; ++s)
#pragma unroll
        for (int d = 0; d < 4; ++d) {
          const unsigned a = ((sb + woff) ^ (64 * d)) + 4096 * s;
          acc[d] = MFMA32(lds_tr2(a, a + 1024), xf[s], acc[d]);
        }
      next();
    }
    bf16x8 qf[8];
#pragma unroll
    for (int s = 0; s < 8; ++s) qf[s] = pack8(acc[s >> 1], s & 1);
#pragma unroll
    for (int d = 0; d < 4; ++d) acc[d] = f32x16{};
    float m = -__builtin_inff(), l = 0.f;
    for (int t = 0; t < 8; ++t) {
      BAR();
      f32x16 s0 = {}, s1 = {};
#pragma unroll
      for (int s = 0; s < 8; ++s) {
        const unsigned a = (sb + koff) ^ (32 * s);
        s0 = MFMA32(lds_b128(a), qf[s], s0);
        s1 = MFMA32(lds_b128(a + 8192), qf[s], s1);
      }
      next();
      float mx = fmaxf(s0[0], s1[0]);
#pragma unroll
      for (int i = 1; i < 16; ++i) mx = fmaxf(mx, fmaxf(s0[i], s1[i]));
      mx = xhalf_max(mx);
      if (__builtin_expect(__any(mx > m + THR_RAW), 0)) {
        const float mn = fmaxf(m, mx), al = __builtin_amdgcn_exp2f((m - mn) * SC_LOG2E);
        m = mn; l *= al;
#pragma unroll
        for (int d = 0; d < 4; ++d) acc[d] *= al;
      }
      const float mc = -m * SC_LOG2E;
      float ps = 0.f;
#pragma unroll
      for (int i = 0; i < 16; ++i) { s0[i] = __builtin_amdgcn_exp2f(fmaf(s0[i], SC_LOG2E, mc)); ps += s0[i]; }
#pragma unroll
      for (int i = 0; i < 16; ++i) { s1[i] = __builtin_amdgcn_exp2f(fmaf(s1[i], SC_LOG2E, mc)); ps += s1[i]; }
      l += ps;
      bf16x8 pf[4] = {pack8(s0, 0), pack8(s0, 1), pack8(s1, 0), pack8(s1, 1)};
      BAR();
#pragma unroll
      for (int s = 0; s < 4; ++s)
#pragma unroll
        for (int d = 0; d < 4; ++d) {
          const unsigned a = ((sb + voff) ^ (64 * d)) + 4096 * s;
          acc[d] = MFMA32(lds_tr2(a, a + 2048), pf[s], acc[d]);
        }
      next();
    }
    const float inv = 1.0f / xhalf_sum(l);
    char* op = ows + ((size_t)((blk * NB + b) * NH + h) * 24 + (2 * wid + (r >> 4))) * 1024 + ((r & 15) + 16 * hh) * 16;
#pragma unroll
    for (int d = 0; d < 4; ++d)
#pragma unroll
      for (int s = 0; s < 2; ++s) *(bf16x8*)(op + d * 6144 + s * 512) = pack8(acc[d], s, inv);
  }
}

constexpr unsigned OSTG = 12288, ONST = 3;
DI void dma_asm(__amdgpu_buffer_rsrc_t r, unsigned lds, unsigned voff, unsigned soff) {
  asm volatile("s_mov_b32 m0, %0\n\ts_nop 0\n\tbuffer_load_dwordx4 %1, %2, %3 offen lds" :: "s"(lds), "v"(voff), "s"(r), "s"(soff) : "memory");
}
__global__ __launch_bounds__(256, 1) void oproj(const char* ows, const char* woA, const char* woB, char* out) {
  extern __shared__ __attribute__((aligned(16))) char lds[];
  const unsigned ldsb = (unsigned)(uintptr_t)(LAS char*)lds;
  const unsigned wid = rfl(threadIdx.x >> 6), lane = threadIdx.x & 63;
  const unsigned id = blockIdx.x, xcd = id & 7, idx = id >> 3, g = 4 * xcd + (idx >> 3), rh = idx & 1, ct = (idx >> 1) & 3, blk = g >> 4;
  const __amdgpu_buffer_rsrc_t ro = mkrsrc(ows + (size_t)g * NH * 24576, NH * 24576);
  const __amdgpu_buffer_rsrc_t rw = mkrsrc(blk ? woB : woA, HD * DM * 2);
  unsigned vw[9];
#pragma unroll
  for (int p = 0; p < 9; ++p) { const unsigned c = 64 * p + lane; vw[p] = (c / 18) * (DM * 2) + (c % 18) * 16; }
  const unsigned vo = lane * 16;
  const unsigned ring = rfl(ldsb + wid * (OSTG * ONST));
  auto issue = [&](int ks) {
    const unsigned kg = wid * 32 + ks, dst = ring + (unsigned)(ks % ONST) * OSTG;
#pragma unroll
    for (int p = 0; p < 3; ++p) dma_asm(ro, rfl(dst + p * 1024), vo, rfl(kg * 6144 + rh * 3072 + p * 1024));
#pragma unroll
    for (int p = 0; p < 9; ++p) dma_asm(rw, rfl(dst + 3072 + p * 1024), vw[p], rfl(kg * 32 * (DM * 2) + ct * 288));
  };
  const unsigned gq = lane >> 4, q4 = (lane & 15) >> 2, p4 = lane & 3;
  const unsigned woff = 3072 + (16 * (gq >> 1) + 4 * (gq & 1) + q4) * 288 + 8 * p4;
  f32x4 acc[3][9];
#pragma unroll
  for (int i = 0; i < 3; ++i)
#pragma unroll
    for (int j = 0; j < 9; ++j) acc[i][j] = f32x4{};
  issue(0); issue(1); issue(2);
  unsigned st = 0;
  for (int ks = 0; ks < 32; ++ks) {
    if (ks < 30) asm volatile("s_waitcnt vmcnt(24)" ::: "memory");
    else if (ks == 30) asm volatile("s_waitcnt vmcnt(12)" ::: "memory");
    else asm volatile("s_waitcnt vmcnt(0)" ::: "memory");
    const unsigned sb = ring + st * OSTG;
    bf16x8 of[3], wf[9];
#pragma unroll
    for (int i = 0; i < 3; ++i) of[i] = lds_b128(sb + vo + i * 1024);
#pragma unroll
    for (int j = 0; j < 9; ++j) wf[j] = lds_tr2(sb + woff + 32 * j, sb + woff + 32 * j + 8 * 288);
    asm volatile("s_waitcnt lgkmcnt(0)" : "+v"(of[0]), "+v"(of[1]), "+v"(of[2]), "+v"(wf[0]), "+v"(wf[1]), "+v"(wf[2]), "+v"(wf[3]), "+v"(wf[4]), "+v"(wf[5]), "+v"(wf[6]), "+v"(wf[7]), "+v"(wf[8]) :: "memory");
    if (ks + 3 < 32) issue(ks + 3);
#pragma unroll
    for (int j = 0; j < 9; ++j)
#pragma unroll
      for (int i = 0; i < 3; ++i) acc[i][j] = MFMA16(wf[j], of[i], acc[i][j]);
    st = st + 1 == ONST ? 0 : st + 1;
  }
#pragma unroll
  for (int i = 0; i < 3; ++i)
#pragma unroll
    for (int j = 0; j < 9; ++j) *(LAS f32x4*)(uintptr_t)(ring + (i * 9 + j) * 1024 + vo) = acc[i][j];
  __syncthreads();
  const unsigned b = g & 15;
  for (unsigned it = threadIdx.x; it < 48 * 36; it += 256) {
    const unsigned q = it / 36, n4 = it % 36;
    const unsigned a = ldsb + ((q >> 4) * 9 + (n4 >> 2)) * 1024 + ((q & 15) + 16 * (n4 & 3)) * 16;
    f32x4 s = *(const LAS f32x4*)(uintptr_t)a;
#pragma unroll
    for (int w = 1; w < 4; ++w) s += *(const LAS f32x4*)(uintptr_t)(a + w * (OSTG * ONST));
    u32x2 o = {cvtpk(s[0], s[1]), cvtpk(s[2], s[3])};
    *(u32x2*)(out + ((size_t)(b * SQ + rh * 48 + q) * (2 * DM) + blk * DM + ct * 144 + n4 * 4) * 2) = o;
  }
}

extern "C" void kernel_launch(void* const* d_in, const int* in_sizes, int n_in, void* d_out, int out_size, void* d_ws, size_t ws_size,
                              hipStream_t stream) {
  (void)in_sizes; (void)n_in; (void)out_size; (void)ws_size;
  (void)hipFuncSetAttribute((const void*)attn_main, hipFuncAttributeMaxDynamicSharedMemorySize, NSLOT * SLOT);
  (void)hipFuncSetAttribute((const void*)oproj, hipFuncAttributeMaxDynamicSharedMemorySize, 4 * OSTG * ONST);
  attn_main<<<512, 256, NSLOT * SLOT, stream>>>((const char*)d_in[0], (const char*)d_in[1], (const char*)d_in[2], (const char*)d_in[3],
                                                (const char*)d_in[5], (const char*)d_in[6], (const char*)d_in[7], (const char*)d_in[8], (char*)d_ws);
  oproj<<<256, 256, 4 * OSTG * ONST, stream>>>((const char*)d_ws, (const char*)d_in[4], (const char*)d_in[9], (char*)d_out);
}
```

```cpp
#include <hip/hip_runtime.h>
#include <hip/hip_bf16.h>
#include <stdint.h>

#define LAS __attribute__((address_space(3)))
#define DI __device__ __forceinline__
typedef __bf16 bf16x2 __attribute__((ext_vector_type(2)));
typedef __bf16 bf16x4 __attribute__((ext_vector_type(4)));
typedef __bf16 bf16x8 __attribute__((ext_vector_type(8)));
typedef float f32x2 __attribute__((ext_vector_type(2)));
typedef float f32x4 __attribute__((ext_vector_type(4)));
typedef float f32x16 __attribute__((ext_vector_type(16)));
typedef unsigned u32x4 __attribute__((ext_vector_type(4)));
typedef unsigned u32x2 __attribute__((ext_vector_type(2)));

constexpr int NB = 16, SQ = 96, DM = 576, NH = 32, SK = 512, DH = 128, HD = NH * DH;
constexpr unsigned SLOT = 32768, NSLOT = 5, LDS_MAIN = SLOT * NSLOT;
constexpr float SC_LOG2E = 0.08838834764831845f * 1.4426950408889634f;
constexpr float THR = 8.0f;

DI unsigned rfl(unsigned x) { return __builtin_amdgcn_readfirstlane(x); }
DI unsigned cvtpk(float lo, float hi) { f32x2 v = {lo, hi}; bf16x2 b = __builtin_convertvector(v, bf16x2); return __builtin_bit_cast(unsigned, b); }
DI bf16x8 pack2(const f32x4& a, const f32x4& b) {
  u32x4 w = {cvtpk(a[0], a[1]), cvtpk(a[2], a[3]), cvtpk(b[0], b[1]), cvtpk(b[2], b[3])};
  return __builtin_bit_cast(bf16x8, w);
}
DI bf16x8 lds_b128(unsigned a) { return *(const LAS bf16x8*)(uintptr_t)a; }
DI bf16x8 lds_tr2(unsigned a0, unsigned a1) {
  bf16x4 lo = __builtin_amdgcn_ds_read_tr16_b64_v4bf16((LAS bf16x4*)(uintptr_t)a0);
  bf16x4 hi = __builtin_amdgcn_ds_read_tr16_b64_v4bf16((LAS bf16x4*)(uintptr_t)a1);
  return __builtin_shufflevector(lo, hi, 0, 1, 2, 3, 4, 5, 6, 7);
}
DI float max2f(float a, float b) { return __builtin_elementwise_maximum(a, b); }
DI float max3f(float a, float b, float c) { return max2f(max2f(a, b), c); }
DI float xg_max(float v) {
  auto r = __builtin_amdgcn_permlane32_swap(__float_as_uint(v), __float_as_uint(v), false, false); v = max2f(__uint_as_float(r[0]), __uint_as_float(r[1]));
  auto s = __builtin_amdgcn_permlane16_swap(__float_as_uint(v), __float_as_uint(v), false, false); return max2f(__uint_as_float(s[0]), __uint_as_float(s[1]));
}
DI float xg_sum(float v) {
  auto r = __builtin_amdgcn_permlane32_swap(__float_as_uint(v), __float_as_uint(v), false, false); v = __uint_as_float(r[0]) + __uint_as_float(r[1]);
  auto s = __builtin_amdgcn_permlane16_swap(__float_as_uint(v), __float_as_uint(v), false, false); return __uint_as_float(s[0]) + __uint_as_float(s[1]);
}
#define MFMA16(a, b, c) __builtin_amdgcn_mfma_f32_16x16x32_bf16((a), (b), (c), 0, 0, 0)
#define SB0() __builtin_amdgcn_sched_barrier(0)
#define WAITBAR(n) do { SB0(); __builtin_amdgcn_s_waitcnt(0xC07F); asm volatile("s_waitcnt vmcnt(" #n ")\n\ts_barrier" ::: "memory"); SB0(); } while (0)
DI __amdgpu_buffer_rsrc_t mkrsrc(const void* p, unsigned bytes) { return __builtin_amdgcn_make_buffer_rsrc((void*)p, (short)0, (int)bytes, 0x00020000); }
DI void dma4_nt(__amdgpu_buffer_rsrc_t r, unsigned lds, unsigned va, unsigned vb, unsigned so) {
  asm volatile("s_mov_b32 m0, %0\n\ts_nop 0\n\t"
               "buffer_load_dwordx4 %1, %3, %4 offen nt lds\n\t"
               "buffer_load_dwordx4 %1, %3, %4 offen offset:1024 nt lds\n\t"
               "buffer_load_dwordx4 %2, %3, %4 offen offset:2048 nt lds\n\t"
               "buffer_load_dwordx4 %2, %3, %4 offen offset:3072 nt lds"
               :: "s"(lds), "v"(va), "v"(vb), "s"(r), "s"(so) : "memory");
}
DI void dma4(__amdgpu_buffer_rsrc_t r, unsigned lds, unsigned v0, unsigned v1, unsigned v2, unsigned v3, unsigned so) {
  asm volatile("s_mov_b32 m0, %0\n\ts_nop 0\n\t"
               "buffer_load_dwordx4 %1, %5, %6 offen lds\n\t"
               "buffer_load_dwordx4 %2, %5, %6 offen offset:1024 lds\n\t"
               "buffer_load_dwordx4 %3, %5, %6 offen offset:2048 lds\n\t"
               "buffer_load_dwordx4 %4, %5, %6 offen offset:3072 lds"
               :: "s"(lds), "v"(v0), "v"(v1), "v"(v2), "v"(v3), "s"(r), "s"(so) : "memory");
}
DI void dma1(__amdgpu_buffer_rsrc_t r, unsigned lds, unsigned v, unsigned so) {
  asm volatile("s_mov_b32 m0, %0\n\ts_nop 0\n\tbuffer_load_dwordx4 %1, %2, %3 offen lds" :: "s"(lds), "v"(v), "s"(r), "s"(so) : "memory");
}

DI void dma3(__amdgpu_buffer_rsrc_t r, unsigned lds, unsigned v0, unsigned v1, unsigned v2, unsigned so) {
  asm volatile("s_mov_b32 m0, %0\n\ts_nop 0\n\t"
               "buffer_load_dwordx4 %1, %4, %5 offen lds\n\t"
               "buffer_load_dwordx4 %2, %4, %5 offen offset:1024 lds\n\t"
               "buffer_load_dwordx4 %3, %4, %5 offen offset:2048 lds"
               :: "s"(lds), "v"(v0), "v"(v1), "v"(v2), "s"(r), "s"(so) : "memory");
}
#define BARRIER() do { SB0(); __builtin_amdgcn_s_waitcnt(0xC07F); asm volatile("s_barrier" ::: "memory"); SB0(); } while (0)
#define WAITV(n) asm volatile("s_waitcnt vmcnt(" #n ")" ::: "memory")

constexpr unsigned KVS = 16384, NKV = 4;
constexpr unsigned QBUF = 45056;
constexpr unsigned OREG = 65536, WREG = 114688, WOU = 12288;
constexpr unsigned PART = 110592;

DI bf16x8 pack_swap(const f32x4& a0, const f32x4& a1) {
  unsigned p00 = cvtpk(a0[0], a0[1]), p01 = cvtpk(a0[2], a0[3]), p10 = cvtpk(a1[0], a1[1]), p11 = cvtpk(a1[2], a1[3]);
  auto r0 = __builtin_amdgcn_permlane32_swap(p00, p10, false, false);
  auto r1 = __builtin_amdgcn_permlane32_swap(p01, p11, false, false);
  auto t0 = __builtin_amdgcn_permlane16_swap(r0[0], r0[1], false, false);
  auto t1 = __builtin_amdgcn_permlane16_swap(r1[0], r1[1], false, false);
  u32x4 w = {t0[0], t1[0], t0[1], t1[1]};
  return __builtin_bit_cast(bf16x8, w);
}

__global__ __launch_bounds__(512, 2) void attn_main(const char* xA, const char* kA, const char* vA, const char* wqA, const char* woA,
                                                    const char* xB, const char* kB, const char* vB, const char* wqB, const char* woB, char* ows) {
  extern __shared__ __attribute__((aligned(16))) char lds[];
  const unsigned ldsb = rfl((unsigned)(uintptr_t)(LAS char*)lds);
  const unsigned wid = rfl(threadIdx.x >> 6), lane = threadIdx.x & 63, team = wid >> 2, u = wid & 3, jj = u >> 1, qh = u & 1;
  const unsigned id = blockIdx.x, xcd = id & 7, idx = id >> 3, blk = xcd >> 2;
  const unsigned b = idx & 15, hb = 8 * (xcd & 3) + 4 * (idx >> 4), h = hb + 2 * team + jj;
  const char* kp = blk ? kB : kA; const char* vp = blk ? vB : vA;
  const __amdgpu_buffer_rsrc_t rk = mkrsrc(kp + (size_t)(b * NH + hb) * SK * DH * 2, 4 * SK * DH * 2);
  const __amdgpu_buffer_rsrc_t rv = mkrsrc(vp + (size_t)(b * NH + hb) * SK * DH * 2, 4 * SK * DH * 2);
  const unsigned g = lane >> 4, c16 = lane & 15, q4 = c16 >> 2, p4 = lane & 3;
  const unsigned vka = g * 256 + 16 * (c16 ^ ((8 * qh) | g)), vkb = g * 256 + 16 * (c16 ^ ((8 * qh + 4) | g));
  const unsigned vva = g * 256 + 16 * (c16 ^ (g << 2)), vvb = g * 256 + 16 * (c16 ^ ((g << 2) | 2));
  const unsigned krot = (id * 5 + (id >> 3) + jj * 7) & 15;
  auto kv_issue = [&](unsigned e) {
    const unsigned so = ((e >> 5) * 2 + jj) * (SK * DH * 2) + ((((e >> 1) & 15) + krot) & 15) * 8192 + 4096 * qh;
    const unsigned dst = ldsb + (e % NKV) * KVS + 8192 * jj + 4096 * qh;
    if (e & 1) dma4_nt(rv, dst, vva, vvb, so); else dma4_nt(rk, dst, vka, vkb, so);
  };
  const unsigned tro = 256 * (8 * g + q4) + 16 * ((q4 << 2) | ((g & 1) << 1) | (p4 >> 1)) + 8 * (p4 & 1);
  const unsigned xo0 = 32768 + (48 * qh + c16) * 128 + 16 * (g ^ ((c16 >> 1) & 7));
  auto rd_tr8 = [&](bf16x8 (&f)[8], unsigned a) {
#pragma unroll
    for (int d = 0; d < 8; ++d) f[d] = lds_tr2(a ^ (32 * d), (a ^ (32 * d)) + 1024);
  };

  bf16x8 qf[4][3];
  f32x4 acc[8][3];
  auto zero_acc = [&]() {
#pragma unroll
    for (int d = 0; d < 8; ++d)
#pragma unroll
      for (int q = 0; q < 3; ++q) acc[d][q] = f32x4{};
  };
  auto pack_q = [&]() {
#pragma unroll
    for (int s = 0; s < 4; ++s)
#pragma unroll
      for (int q = 0; q < 3; ++q) qf[s][q] = pack_swap(acc[2 * s][q] * SC_LOG2E, acc[2 * s + 1][q] * SC_LOG2E);
  };

  if (team == 0) {
    zero_acc();
    bf16x8 wa[8], wb[8], xa[3], xb[3], xc[3];
#pragma unroll
    for (int d = 0; d < 8; ++d) wb[d] = bf16x8{};
#pragma unroll
    for (int q = 0; q < 3; ++q) xc[q] = bf16x8{};
    unsigned bufb = ldsb;
#pragma clang loop unroll(disable)
    for (int c = 0; c < 9; ++c) {
      BARRIER();
      const unsigned a = bufb + 16384 * jj + tro, xo = bufb + xo0;
      bufb = bufb == ldsb + 2 * QBUF ? ldsb : bufb + QBUF;
#pragma unroll
      for (int d = 0; d < 8; ++d) {
#pragma unroll
        for (int q = 0; q < 3; ++q) acc[d][q] = MFMA16(wb[d], xc[q], acc[d][q]);
        if (d == 0) { xa[0] = lds_b128(xo); xa[1] = lds_b128(xo + 2048); xa[2] = lds_b128(xo + 4096); }
        if (d >= 1 && d < 5) { wa[2 * d - 2] = lds_tr2(a ^ (64 * d - 64), (a ^ (64 * d - 64)) + 1024); wa[2 * d - 1] = lds_tr2(a ^ (64 * d - 32), (a ^ (64 * d - 32)) + 1024); }
        if (d == 5) { xb[0] = lds_b128(xo ^ 64); xb[1] = lds_b128((xo ^ 64) + 2048); xb[2] = lds_b128((xo ^ 64) + 4096); }
        SB0();
      }
#pragma unroll
      for (int d = 0; d < 8; ++d) {
#pragma unroll
        for (int q = 0; q < 3; ++q) acc[d][q] = MFMA16(wa[d], xa[q], acc[d][q]);
        if (d < 4) { wb[2 * d] = lds_tr2((a ^ (64 * d)) + 8192, (a ^ (64 * d)) + 9216); wb[2 * d + 1] = lds_tr2((a ^ (64 * d + 32)) + 8192, (a ^ (64 * d + 32)) + 9216); }
        SB0();
      }
#pragma unroll
      for (int q = 0; q < 3; ++q) xc[q] = xb[q];
    }
#pragma unroll
    for (int d = 0; d < 8; ++d)
#pragma unroll
      for (int q = 0; q < 3; ++q) acc[d][q] = MFMA16(wb[d], xc[q], acc[d][q]);
    pack_q();
  } else {
    const char* xp = blk ? xB : xA; const char* wp = blk ? wqB : wqA;
    const __amdgpu_buffer_rsrc_t rx = mkrsrc(xp + (size_t)b * SQ * DM * 2, SQ * DM * 2);
    const __amdgpu_buffer_rsrc_t rw = mkrsrc(wp, DM * HD * 2);
    unsigned vw[4], vx[3];
#pragma unroll
    for (int pp = 0; pp < 4; ++pp) vw[pp] = (4 * pp + g) * (HD * 2) - 1024 * pp + 16 * (c16 ^ ((g << 2) | (pp & 2)));
#pragma unroll
    for (int i = 0; i < 3; ++i) vx[i] = (8 * i + (lane >> 3)) * (DM * 2) - 1024 * i + 16 * ((lane & 7) ^ ((4 * (3 * u + i) + g) & 7));
    const unsigned qrot = (b + 4 * (idx >> 4)) % 9;
    auto xw_issue = [&](unsigned cc, unsigned buf, unsigned head) {
      unsigned c = cc + qrot; if (c >= 9) c -= 9;
      dma3(rx, buf + 32768 + 3072 * u, vx[0], vx[1], vx[2], c * 128 + 24 * u * (DM * 2));
      const unsigned so = (c * 64 + 32 * qh) * (HD * 2) + head * 256, dst = buf + 16384 * jj + 8192 * qh;
      dma4(rw, dst, vw[0], vw[1], vw[2], vw[3], so);
      dma4(rw, dst + 4096, vw[0], vw[1], vw[2], vw[3], so + 16 * (HD * 2));
    };
    xw_issue(0, ldsb, h - 2); xw_issue(1, ldsb + QBUF, h - 2);
    unsigned bufb = ldsb + 2 * QBUF;
#pragma clang loop unroll(disable)
    for (int c = 0; c < 7; ++c) {
      WAITV(11); BARRIER();
      xw_issue(c + 2, bufb, h - 2);
      bufb = bufb == ldsb + 2 * QBUF ? ldsb : bufb + QBUF;
    }
    WAITV(11); BARRIER();
    kv_issue(0); kv_issue(1);
    WAITV(8); BARRIER();
    kv_issue(2);
    zero_acc();
    const unsigned qb = ldsb + 2 * QBUF;
    bf16x8 wf[2][8], xf[2][3];
    auto third = [&](int n) {
#pragma unroll
      for (int m = 16 * n; m < 16 * n + 16; ++m) { const int ks = m / 24, d = (m % 24) / 3, q = m % 3; acc[d][q] = MFMA16(wf[ks][d], xf[ks][q], acc[d][q]); }
    };
    unsigned e = 0;
#pragma clang loop unroll(disable)
    for (int c = 0; c < 10; ++c) {
      WAITV(8); BARRIER();
      if (c < 9) xw_issue(c, qb, h);
      kv_issue(e + 3); ++e;
      if (c > 0) third(0);
      if (c < 9) WAITV(19); else WAITV(8);
      BARRIER();
      kv_issue(e + 3); ++e;
      if (c > 0) third(1);
      WAITV(8); BARRIER();
      kv_issue(e + 3); ++e;
      if (c > 0) third(2);
      SB0();
      if (c < 9) {
#pragma unroll
        for (int ks = 0; ks < 2; ++ks) {
#pragma unroll
          for (int q = 0; q < 3; ++q) xf[ks][q] = lds_b128(((qb + xo0) ^ (64 * ks)) + 2048 * q);
          rd_tr8(wf[ks], qb + 16384 * jj + 8192 * ks + tro);
        }
      }
    }
    WAITV(8); BARRIER();
    kv_issue(33);
    pack_q();
    WAITV(8); BARRIER();
    kv_issue(34);
  }

  const char* wop = blk ? woB : woA;
  const __amdgpu_buffer_rsrc_t ro = mkrsrc(wop, HD * DM * 2);
  const unsigned prot = (b >> 3) + (b & 1), ksrot = b & 7;
  unsigned vo[3];
#pragma unroll
  for (int i = 0; i < 3; ++i) { const unsigned L = 64 * i + lane, row = L / 24, ph = L - 24 * row; vo[i] = row * (DM * 2) + 16 * (ph ^ (row & 6)) - 1024 * i; }
  auto wo_issue = [&](unsigned i, unsigned buf) {
    const unsigned r = i >= 24, k = i - 24 * r; unsigned p = (k >> 3) + prot; if (p >= 3) p -= 3;
    dma3(ro, buf + 3072 * u, vo[0], vo[1], vo[2], ((hb + 2 * r) * DH + 32 * ((k + ksrot) & 7) + 8 * u) * (DM * 2) + 384 * p);
  };

  {
    zero_acc();
    f32x4 negm[3] = {f32x4{}, f32x4{}, f32x4{}};
    float l[3] = {0.f, 0.f, 0.f};
    const unsigned koff = ldsb + 8192 * jj + 256 * (8 * q4 + p4) + 16 * (g ^ c16), voff = ldsb + KVS + 8192 * jj + tro;
#pragma clang loop unroll(disable)
    for (int t = 0; t < 16; ++t) {
      if (t == 0) WAITV(4); else WAITV(0);
      BARRIER();
      const unsigned sb = (t & 1) * (2 * KVS);
      bf16x8 kf[2][4];
#pragma unroll
      for (int s = 0; s < 4; ++s) { const unsigned a = (sb + koff) ^ (64 * s); kf[0][s] = lds_b128(a); kf[1][s] = lds_b128(a + 1024); }
      SB0();
      f32x4 sc[2][3];
#pragma unroll
      for (int kb = 0; kb < 2; ++kb)
#pragma unroll
        for (int q = 0; q < 3; ++q) sc[kb][q] = MFMA16(kf[kb][0], qf[0][q], negm[q]);
#pragma unroll
      for (int s = 1; s < 4; ++s)
#pragma unroll
        for (int kb = 0; kb < 2; ++kb)
#pragma unroll
          for (int q = 0; q < 3; ++q) sc[kb][q] = MFMA16(kf[kb][s], qf[s][q], sc[kb][q]);
      float mx[3];
#pragma unroll
      for (int q = 0; q < 3; ++q)
        mx[q] = max3f(max3f(sc[0][q][0], sc[0][q][1], sc[0][q][2]), max3f(sc[0][q][3], sc[1][q][0], sc[1][q][1]), max2f(sc[1][q][2], sc[1][q][3]));
      if (t == 0 || __builtin_expect(__any(!(max3f(mx[0], mx[1], mx[2]) <= THR)), 0)) {
#pragma unroll
        for (int q = 0; q < 3; ++q) {
          float dl = xg_max(mx[q]);
          if (t != 0) dl = max2f(dl, 0.f);
          const float al = t == 0 ? 0.f : __builtin_amdgcn_exp2f(-dl);
          negm[q] -= dl; sc[0][q] -= dl; sc[1][q] -= dl; l[q] *= al;
#pragma unroll
          for (int d = 0; d < 8; ++d) acc[d][q] *= al;
        }
      }
      bf16x8 pf[3];
#pragma unroll
      for (int q = 0; q < 3; ++q) {
#pragma unroll
        for (int i = 0; i < 4; ++i) { sc[0][q][i] = __builtin_amdgcn_exp2f(sc[0][q][i]); sc[1][q][i] = __builtin_amdgcn_exp2f(sc[1][q][i]); }
        l[q] += ((sc[0][q][0] + sc[0][q][1]) + (sc[0][q][2] + sc[0][q][3])) + ((sc[1][q][0] + sc[1][q][1]) + (sc[1][q][2] + sc[1][q][3]));
        pf[q] = pack2(sc[0][q], sc[1][q]);
      }
      BARRIER();
      bf16x8 vf[8];
      rd_tr8(vf, sb + voff);
      SB0();
#pragma unroll
      for (int d = 0; d < 8; ++d)
#pragma unroll
        for (int q = 0; q < 3; ++q) acc[d][q] = MFMA16(vf[d], pf[q], acc[d][q]);
    }
    if (team == 0) { BARRIER(); wo_issue(0, ldsb + WREG); kv_issue(35); }
#pragma unroll
    for (int q = 0; q < 3; ++q) {
      const float inv = 1.0f / xg_sum(l[q]);
#pragma unroll
      for (int s = 0; s < 4; ++s)
        *(LAS bf16x8*)(uintptr_t)(ldsb + OREG + ((4 * jj + s) * 6 + 3 * qh + q) * 1024 + lane * 16) = pack2(acc[2 * s][q] * inv, acc[2 * s + 1][q] * inv);
    }
  }

  if (team == 0) {
    unsigned wof[6];
#pragma unroll
    for (int n = 0; n < 6; ++n) wof[n] = (4 * g + q4) * 384 + 16 * ((12 * jj + 2 * n + (p4 >> 1)) ^ ((4 * g + q4) & 6)) + 8 * (p4 & 1);
    const unsigned oof = ldsb + OREG + 3 * qh * 1024 + lane * 16;
    f32x4 oc[6][3];
    u32x2 pA[6][3], pB[6][3], pC[6][3];
#pragma unroll
    for (int n = 0; n < 6; ++n)
#pragma unroll
      for (int q = 0; q < 3; ++q) { oc[n][q] = f32x4{}; pA[n][q] = u32x2{}; pB[n][q] = u32x2{}; pC[n][q] = u32x2{}; }
#pragma clang loop unroll(disable)
    for (unsigned e = 33; e < 61; ++e) {
      WAITV(14); BARRIER();
      if (e < 60) wo_issue(e - 32, ldsb + WREG + (e & 3) * WOU);
      kv_issue(e + 3);
      const unsigned i = e - 35;
      if (i < 24) {
        const unsigned wb = ldsb + WREG + (i & 3) * WOU, ob = oof + ((i + ksrot) & 7) * 6144;
        bf16x8 of[3], wf[6];
#pragma unroll
        for (int q = 0; q < 3; ++q) of[q] = lds_b128(ob + 1024 * q);
#pragma unroll
        for (int n = 0; n < 6; ++n) wf[n] = lds_tr2(wb + wof[n], wb + wof[n] + 16 * 384);
#pragma unroll
        for (int n = 0; n < 6; ++n)
#pragma unroll
          for (int q = 0; q < 3; ++q) oc[n][q] = MFMA16(wf[n], of[q], oc[n][q]);
        if ((i & 7) == 7) {
#pragma unroll
          for (int n = 0; n < 6; ++n)
#pragma unroll
            for (int q = 0; q < 3; ++q) {
              pA[n][q] = pB[n][q]; pB[n][q] = pC[n][q];
              pC[n][q] = u32x2{cvtpk(oc[n][q][0], oc[n][q][1]), cvtpk(oc[n][q][2], oc[n][q][3])};
              oc[n][q] = f32x4{};
            }
        }
      }
    }
    WAITV(11); BARRIER();
    WAITV(4); BARRIER();
    WAITV(0); BARRIER();
#pragma unroll
    for (int k = 4; k < 8; ++k) wo_issue(24 + k, ldsb + (k & 3) * WOU);
    char* pp = ows + (size_t)((blk * NB + b) * 8 + (hb >> 2)) * PART + u * 9216 + lane * 16;
    auto unpark = [&]() {
#pragma unroll
      for (int n = 0; n < 6; ++n)
#pragma unroll
        for (int q = 0; q < 3; ++q) {
          oc[n][q] = f32x4{__uint_as_float(pA[n][q][0] << 16), __uint_as_float(pA[n][q][0] & 0xffff0000u), __uint_as_float(pA[n][q][1] << 16), __uint_as_float(pA[n][q][1] & 0xffff0000u)};
          pA[n][q] = pB[n][q]; pB[n][q] = pC[n][q];
        }
    };
    auto store_pass = [&](unsigned pi) {
      unsigned p = pi + prot; if (p >= 3) p -= 3;
#pragma unroll
      for (int n = 0; n < 3; ++n)
#pragma unroll
        for (int q = 0; q < 3; ++q) *(bf16x8*)(pp + p * 36864 + (n * 3 + q) * 1024) = pack_swap(oc[2 * n][q], oc[2 * n + 1][q]);
    };
    unpark();
    bf16x8 wa[6], wb[6], oa[3], ob[3];
#pragma unroll
    for (int n = 0; n < 6; ++n) wb[n] = bf16x8{};
#pragma unroll
    for (int q = 0; q < 3; ++q) ob[q] = bf16x8{};
#pragma clang loop unroll(disable)
    for (unsigned j = 0; j < 12; ++j) {
      if (j == 2) WAITV(0);
      BARRIER();
      const unsigned k0 = 2 * j, buf = ldsb + ((k0 & 4) ? 0 : WREG) + (k0 & 3) * WOU;
      const unsigned o0 = oof + ((k0 + ksrot) & 7) * 6144, o1 = oof + ((k0 + 1 + ksrot) & 7) * 6144;
#pragma unroll
      for (int n = 0; n < 6; ++n) {
#pragma unroll
        for (int q = 0; q < 3; ++q) oc[n][q] = MFMA16(wb[n], ob[q], oc[n][q]);
        if (n == 0) { oa[0] = lds_b128(o0); oa[1] = lds_b128(o0 + 1024); oa[2] = lds_b128(o0 + 2048); }
        if (n >= 1 && n < 4) { wa[2 * n - 2] = lds_tr2(buf + wof[2 * n - 2], buf + wof[2 * n - 2] + 16 * 384); wa[2 * n - 1] = lds_tr2(buf + wof[2 * n - 1], buf + wof[2 * n - 1] + 16 * 384); }
        SB0();
      }
      if (j == 4 || j == 8) { store_pass((j >> 2) - 1); unpark(); }
#pragma unroll
      for (int n = 0; n < 6; ++n) {
#pragma unroll
        for (int q = 0; q < 3; ++q) oc[n][q] = MFMA16(wa[n], oa[q], oc[n][q]);
        if (n == 0) { ob[0] = lds_b128(o1); ob[1] = lds_b128(o1 + 1024); ob[2] = lds_b128(o1 + 2048); }
        if (n >= 1 && n < 4) { wb[2 * n - 2] = lds_tr2(buf + WOU + wof[2 * n - 2], buf + WOU + wof[2 * n - 2] + 16 * 384); wb[2 * n - 1] = lds_tr2(buf + WOU + wof[2 * n - 1], buf + WOU + wof[2 * n - 1] + 16 * 384); }
        SB0();
      }
    }
#pragma unroll
    for (int n = 0; n < 6; ++n)
#pragma unroll
      for (int q = 0; q < 3; ++q) oc[n][q] = MFMA16(wb[n], ob[q], oc[n][q]);
    store_pass(2);
  } else {
#pragma clang loop unroll(disable)
    for (unsigned j = 0; j < 12; ++j) {
      if (j < 10) WAITV(12); else if (j == 10) WAITV(6); else WAITV(0);
      BARRIER();
      if (j >= 1 && j < 9) {
        const unsigned k = 2 * j + 6, buf = ldsb + ((k & 4) ? 0 : WREG) + (k & 3) * WOU;
        wo_issue(24 + k, buf); wo_issue(25 + k, buf + WOU);
      }
    }
  }
  asm volatile("s_waitcnt vmcnt(0)" ::: "memory");
}

__global__ __launch_bounds__(256) void reduce_out(const char* __restrict__ ows, char* __restrict__ out) {
  const unsigned t = blockIdx.x * 256 + threadIdx.x, grp = t / 6912, r = t - grp * 6912;
  const char* p = ows + (size_t)grp * 8 * PART + r * 16;
  u32x4 v[8];
#pragma unroll
  for (int i = 0; i < 8; ++i) v[i] = *(const u32x4*)(p + i * PART);
  float s[8] = {0.f, 0.f, 0.f, 0.f, 0.f, 0.f, 0.f, 0.f};
#pragma unroll
  for (int i = 0; i < 8; ++i)
#pragma unroll
    for (int k = 0; k < 4; ++k) { s[2 * k] += __uint_as_float(v[i][k] << 16); s[2 * k + 1] += __uint_as_float(v[i][k] & 0xffff0000u); }
  const unsigned lane = r & 63, f = r >> 6, pu = f / 9, i9 = f - 9 * pu, np = i9 / 3, q = i9 - 3 * np, p_ = pu >> 2, u = pu & 3;
  const unsigned row = 48 * (u & 1) + 16 * q + (lane & 15), col = 192 * p_ + 96 * (u >> 1) + 32 * np + 8 * (lane >> 4);
  u32x4 o = {cvtpk(s[0], s[1]), cvtpk(s[2], s[3]), cvtpk(s[4], s[5]), cvtpk(s[6], s[7])};
  *(u32x4*)(out + ((size_t)((grp & 15) * SQ + row) * (2 * DM) + (grp >> 4) * DM + col) * 2) = o;
}

extern "C" void kernel_launch(void* const* d_in, const int* in_sizes, int n_in, void* d_out, int out_size, void* d_ws, size_t ws_size,
                              hipStream_t stream) {
  (void)in_sizes; (void)n_in; (void)out_size; (void)ws_size;
  (void)hipFuncSetAttribute((const void*)attn_main, hipFuncAttributeMaxDynamicSharedMemorySize, LDS_MAIN);
  attn_main<<<256, 512, LDS_MAIN, stream>>>((const char*)d_in[0], (const char*)d_in[1], (const char*)d_in[2], (const char*)d_in[3], (const char*)d_in[4],
                                            (const char*)d_in[5], (const char*)d_in[6], (const char*)d_in[7], (const char*)d_in[8], (const char*)d_in[9], (char*)d_ws);
  reduce_out<<<864, 256, 0, stream>>>((const char*)d_ws, (char*)d_out);
}
```

```cpp
#include <hip/hip_runtime.h>
#include <hip/hip_bf16.h>
#include <stdint.h>

#define LAS __attribute__((address_space(3)))
#define DI __device__ __forceinline__
typedef __bf16 bf16x2 __attribute__((ext_vector_type(2)));
typedef __bf16 bf16x4 __attribute__((ext_vector_type(4)));
typedef __bf16 bf16x8 __attribute__((ext_vector_type(8)));
typedef float f32x2 __attribute__((ext_vector_type(2)));
typedef float f32x4 __attribute__((ext_vector_type(4)));
typedef unsigned u32x4 __attribute__((ext_vector_type(4)));
typedef unsigned u32x2 __attribute__((ext_vector_type(2)));

constexpr int NB = 16, SQ = 96, DM = 576, NH = 32, SK = 512, DH = 128, HD = NH * DH;
constexpr unsigned LDS_MAIN = 163840;
constexpr float SC_LOG2E = 0.08838834764831845f * 1.4426950408889634f;
constexpr float THR = 8.0f;

DI unsigned rfl(unsigned x) { return __builtin_amdgcn_readfirstlane(x); }
DI unsigned cvtpk(float lo, float hi) { f32x2 v = {lo, hi}; bf16x2 b = __builtin_convertvector(v, bf16x2); return __builtin_bit_cast(unsigned, b); }
DI bf16x8 pack2(const f32x4& a, const f32x4& b) {
  u32x4 w = {cvtpk(a[0], a[1]), cvtpk(a[2], a[3]), cvtpk(b[0], b[1]), cvtpk(b[2], b[3])};
  return __builtin_bit_cast(bf16x8, w);
}
DI bf16x8 pack_swap(const f32x4& a0, const f32x4& a1) {
  unsigned p00 = cvtpk(a0[0], a0[1]), p01 = cvtpk(a0[2], a0[3]), p10 = cvtpk(a1[0], a1[1]), p11 = cvtpk(a1[2], a1[3]);
  auto r0 = __builtin_amdgcn_permlane32_swap(p00, p10, false, false);
  auto r1 = __builtin_amdgcn_permlane32_swap(p01, p11, false, false);
  auto t0 = __builtin_amdgcn_permlane16_swap(r0[0], r0[1], false, false);
  auto t1 = __builtin_amdgcn_permlane16_swap(r1[0], r1[1], false, false);
  u32x4 w = {t0[0], t1[0], t0[1], t1[1]};
  return __builtin_bit_cast(bf16x8, w);
}
DI bf16x8 lds_b128(unsigned a) { return *(const LAS bf16x8*)(uintptr_t)a; }
DI bf16x8 lds_tr2(unsigned a0, unsigned a1) {
  bf16x4 lo = __builtin_amdgcn_ds_read_tr16_b64_v4bf16((LAS bf16x4*)(uintptr_t)a0);
  bf16x4 hi = __builtin_amdgcn_ds_read_tr16_b64_v4bf16((LAS bf16x4*)(uintptr_t)a1);
  return __builtin_shufflevector(lo, hi, 0, 1, 2, 3, 4, 5, 6, 7);
}
DI float max2f(float a, float b) { return __builtin_elementwise_maximum(a, b); }
DI float max3f(float a, float b, float c) { return max2f(max2f(a, b), c); }
DI float xg_max(float v) {
  auto r = __builtin_amdgcn_permlane32_swap(__float_as_uint(v), __float_as_uint(v), false, false); v = max2f(__uint_as_float(r[0]), __uint_as_float(r[1]));
  auto s = __builtin_amdgcn_permlane16_swap(__float_as_uint(v), __float_as_uint(v), false, false); return max2f(__uint_as_float(s[0]), __uint_as_float(s[1]));
}
DI float xg_sum(float v) {
  auto r = __builtin_amdgcn_permlane32_swap(__float_as_uint(v), __float_as_uint(v), false, false); v = __uint_as_float(r[0]) + __uint_as_float(r[1]);
  auto s = __builtin_amdgcn_permlane16_swap(__float_as_uint(v), __float_as_uint(v), false, false); return __uint_as_float(s[0]) + __uint_as_float(s[1]);
}
#define MFMA16(a, b, c) __builtin_amdgcn_mfma_f32_16x16x32_bf16((a), (b), (c), 0, 0, 0)
#define SB0() __builtin_amdgcn_sched_barrier(0)
#define MEMFENCE() asm volatile("" ::: "memory")
#define BARRIER() do { SB0(); __builtin_amdgcn_s_waitcnt(0xC07F); asm volatile("s_barrier" ::: "memory"); SB0(); } while (0)
#define WAITV(n) asm volatile("s_waitcnt vmcnt(" #n ")" ::: "memory")
template <int N> DI void weave() {
#pragma unroll
  for (int i = 0; i < N; ++i) { __builtin_amdgcn_sched_group_barrier(0x8, 1, 0); __builtin_amdgcn_sched_group_barrier(0x100, 1, 0); }
}
DI __amdgpu_buffer_rsrc_t mkrsrc(const void* p, unsigned bytes) { return __builtin_amdgcn_make_buffer_rsrc((void*)p, (short)0, (int)bytes, 0x00020000); }
DI void dma4_nt(__amdgpu_buffer_rsrc_t r, unsigned lds, unsigned va, unsigned vb, unsigned so) {
  asm volatile("s_mov_b32 m0, %0\n\ts_nop 0\n\t"
               "buffer_load_dwordx4 %1, %3, %4 offen nt lds\n\t"
               "buffer_load_dwordx4 %1, %3, %4 offen offset:1024 nt lds\n\t"
               "buffer_load_dwordx4 %2, %3, %4 offen offset:2048 nt lds\n\t"
               "buffer_load_dwordx4 %2, %3, %4 offen offset:3072 nt lds"
               :: "s"(lds), "v"(va), "v"(vb), "s"(r), "s"(so) : "memory");
}
DI void dma4(__amdgpu_buffer_rsrc_t r, unsigned lds, unsigned v0, unsigned v1, unsigned v2, unsigned v3, unsigned so) {
  asm volatile("s_mov_b32 m0, %0\n\ts_nop 0\n\t"
               "buffer_load_dwordx4 %1, %5, %6 offen lds\n\t"
               "buffer_load_dwordx4 %2, %5, %6 offen offset:1024 lds\n\t"
               "buffer_load_dwordx4 %3, %5, %6 offen offset:2048 lds\n\t"
               "buffer_load_dwordx4 %4, %5, %6 offen offset:3072 lds"
               :: "s"(lds), "v"(v0), "v"(v1), "v"(v2), "v"(v3), "s"(r), "s"(so) : "memory");
}
DI void dma3(__amdgpu_buffer_rsrc_t r, unsigned lds, unsigned v0, unsigned v1, unsigned v2, unsigned so) {
  asm volatile("s_mov_b32 m0, %0\n\ts_nop 0\n\t"
               "buffer_load_dwordx4 %1, %4, %5 offen lds\n\t"
               "buffer_load_dwordx4 %2, %4, %5 offen offset:1024 lds\n\t"
               "buffer_load_dwordx4 %3, %4, %5 offen offset:2048 lds"
               :: "s"(lds), "v"(v0), "v"(v1), "v"(v2), "s"(r), "s"(so) : "memory");
}
DI void touch(__amdgpu_buffer_rsrc_t r, unsigned lds, unsigned v, unsigned so) {
  asm volatile("s_mov_b32 m0, %0\n\ts_nop 0\n\tbuffer_load_dword %1, %2, %3 offen lds" :: "s"(lds), "v"(v), "s"(r), "s"(so) : "memory");
}

#define STAUX 16
#define LDNT 0
#define WSEND 0
constexpr unsigned KVS = 16384, NKV = 4;
constexpr unsigned QB1 = 73728, QB2 = 118784;
constexpr unsigned SCR = 65536;
constexpr unsigned OREG = 65536, WREG = 114688, WOU = 12288;
constexpr unsigned PART = 110592;

__global__ __launch_bounds__(512, 2) void attn_main(const char* xA, const char* kA, const char* vA, const char* wqA, const char* woA,
                                                    const char* xB, const char* kB, const char* vB, const char* wqB, const char* woB, char* ows) {
  extern __shared__ __attribute__((aligned(16))) char lds[];
  const unsigned ldsb = rfl((unsigned)(uintptr_t)(LAS char*)lds);
  const unsigned wid = rfl(threadIdx.x >> 6), lane = threadIdx.x & 63, team = wid >> 2, u = wid & 3, jj = u >> 1, qh = u & 1;
  const unsigned id = blockIdx.x, xcd = id & 7, idx = id >> 3, blk = xcd >> 2;
  const unsigned b = idx & 15, hb = 8 * (xcd & 3) + 4 * (idx >> 4), h = hb + 2 * team + jj;
  const char* kp = blk ? kB : kA; const char* vp = blk ? vB : vA; const char* wop = blk ? woB : woA;
  const __amdgpu_buffer_rsrc_t rk = mkrsrc(kp + (size_t)(b * NH + hb) * SK * DH * 2, 4 * SK * DH * 2);
  const __amdgpu_buffer_rsrc_t rv = mkrsrc(vp + (size_t)(b * NH + hb) * SK * DH * 2, 4 * SK * DH * 2);
  const __amdgpu_buffer_rsrc_t ro = mkrsrc(wop, HD * DM * 2);
  const unsigned g = lane >> 4, c16 = lane & 15, q4 = c16 >> 2, p4 = lane & 3;
  const unsigned vka = g * 256 + 16 * (c16 ^ ((8 * qh) | g)), vkb = g * 256 + 16 * (c16 ^ ((8 * qh + 4) | g));
  const unsigned vva = g * 256 + 16 * (c16 ^ (g << 2)), vvb = g * 256 + 16 * (c16 ^ ((g << 2) | 2));
  const unsigned krot = (id * 5 + (id >> 3) + jj * 7) & 15;
  auto kv_issue = [&](unsigned e) {
    const unsigned so = ((e >> 5) * 2 + jj) * (SK * DH * 2) + ((((e >> 1) & 15) + krot) & 15) * 8192 + 4096 * qh;
    const unsigned dst = ldsb + ((e + 3) % NKV) * KVS + 8192 * jj + 4096 * qh;
    if (e & 1) dma4_nt(rv, dst, vva, vvb, so); else dma4_nt(rk, dst, vka, vkb, so);
  };
  const unsigned tro = 256 * (8 * g + q4) + 16 * ((q4 << 2) | ((g & 1) << 1) | (p4 >> 1)) + 8 * (p4 & 1);
  const unsigned xo0 = 32768 + (48 * qh + c16) * 128 + 16 * (g ^ ((c16 >> 1) & 7));
  auto rd_tr8 = [&](bf16x8 (&f)[8], unsigned a, unsigned imm) {
#pragma unroll
    for (int d = 0; d < 8; ++d) f[d] = lds_tr2((a ^ (32 * d)) + imm, (a ^ (32 * d)) + imm + 1024);
  };

  bf16x8 qf[4][3];
  f32x4 acc[8][3];
  auto zero_acc = [&]() {
#pragma unroll
    for (int d = 0; d < 8; ++d)
#pragma unroll
      for (int q = 0; q < 3; ++q) acc[d][q] = f32x4{};
  };
  auto mm24 = [&](const bf16x8 (&a)[8], const bf16x8 (&x)[3]) {
#pragma unroll
    for (int d = 0; d < 8; ++d)
#pragma unroll
      for (int q = 0; q < 3; ++q) acc[d][q] = MFMA16(a[d], x[q], acc[d][q]);
  };
  auto pack_q = [&]() {
#pragma unroll
    for (int s = 0; s < 4; ++s)
#pragma unroll
      for (int q = 0; q < 3; ++q) qf[s][q] = pack_swap(acc[2 * s][q] * SC_LOG2E, acc[2 * s + 1][q] * SC_LOG2E);
  };

  if (team == 0) {
    zero_acc();
    bf16x8 wa[8], wb[8], xa[3], xb[3];
#pragma unroll
    for (int d = 0; d < 8; ++d) wb[d] = bf16x8{};
#pragma unroll
    for (int q = 0; q < 3; ++q) xb[q] = bf16x8{};
    unsigned bufb = ldsb;
#pragma clang loop unroll(disable)
    for (int c = 0; c < 9; ++c) {
      const unsigned a = bufb + 16384 * jj + tro, xo = bufb + xo0;
      bufb = bufb == ldsb ? ldsb + QB1 : bufb == ldsb + QB1 ? ldsb + QB2 : ldsb;
      BARRIER();
#pragma unroll
      for (int q = 0; q < 3; ++q) xa[q] = lds_b128(xo + 2048 * q);
      rd_tr8(wa, a, 0);
      mm24(wb, xb);
      weave<19>();
      SB0();
#pragma unroll
      for (int q = 0; q < 3; ++q) xb[q] = lds_b128((xo ^ 64) + 2048 * q);
      rd_tr8(wb, a, 8192);
      mm24(wa, xa);
      weave<19>();
    }
    mm24(wb, xb);
    pack_q();
  } else {
    const char* xp = blk ? xB : xA; const char* wp = blk ? wqB : wqA;
    const __amdgpu_buffer_rsrc_t rx = mkrsrc(xp + (size_t)b * SQ * DM * 2, SQ * DM * 2);
    const __amdgpu_buffer_rsrc_t rw = mkrsrc(wp, DM * HD * 2);
    unsigned vw[4], vx[3];
#pragma unroll
    for (int pp = 0; pp < 4; ++pp) vw[pp] = (4 * pp + g) * (HD * 2) - 1024 * pp + 16 * (c16 ^ ((g << 2) | (pp & 2)));
#pragma unroll
    for (int i = 0; i < 3; ++i) vx[i] = (8 * i + (lane >> 3)) * (DM * 2) - 1024 * i + 16 * ((lane & 7) ^ ((4 * (3 * u + i) + g) & 7));
    const unsigned qrot = (b + 4 * (idx >> 4)) % 9;
    auto xw_issue = [&](unsigned cc, unsigned buf, unsigned head) {
      unsigned c = cc + qrot; if (c >= 9) c -= 9;
      dma3(rx, buf + 32768 + 3072 * u, vx[0], vx[1], vx[2], c * 128 + 24 * u * (DM * 2));
      const unsigned so = (c * 64 + 32 * qh) * (HD * 2) + head * 256, dst = buf + 16384 * jj + 8192 * qh;
      dma4(rw, dst, vw[0], vw[1], vw[2], vw[3], so);
      dma4(rw, dst + 4096, vw[0], vw[1], vw[2], vw[3], so + 16 * (HD * 2));
    };
    xw_issue(0, ldsb, h - 2); xw_issue(1, ldsb + QB1, h - 2); xw_issue(2, ldsb + QB2, h - 2);
    { unsigned c = 3 + jj + qrot; if (c >= 9) c -= 9; touch(rx, ldsb + SCR, (48 * qh + lane) * (DM * 2), c * 128); }
    WAITV(22);
    BARRIER();
    unsigned bufb = ldsb;
#pragma clang loop unroll(disable)
    for (int c = 1; c < 7; ++c) {
      WAITV(11);
      BARRIER();
      xw_issue(c + 2, bufb, h - 2);
      bufb = bufb == ldsb ? ldsb + QB1 : bufb == ldsb + QB1 ? ldsb + QB2 : ldsb;
    }
    kv_issue(0);
    WAITV(15); BARRIER();
    kv_issue(1); kv_issue(2); kv_issue(3);
    {
      const unsigned so = (hb & ~7u) * DH * (DM * 2) + idx * 36864;
      touch(ro, ldsb + SCR, (64 * u + lane) * 128, so); touch(ro, ldsb + SCR, (256 + 8 * u + (lane & 7)) * 128, so);
    }
    WAITV(18); BARRIER();
    xw_issue(0, ldsb + QB1, h);
    zero_acc();
    WAITV(25); BARRIER();
    WAITV(21); BARRIER(); kv_issue(4);
    WAITV(21); BARRIER(); kv_issue(5);
    unsigned e = 3;
#pragma clang loop unroll(disable)
    for (int c = 0; c < 9; ++c) {
      const unsigned qb = ldsb + ((c & 1) ? QB2 : QB1);
      bf16x8 wf[2][8], xf[2][3];
      WAITV(8); BARRIER();
#pragma unroll
      for (int ks = 0; ks < 2; ++ks) {
#pragma unroll
        for (int q = 0; q < 3; ++q) xf[ks][q] = lds_b128(((qb + xo0) ^ (64 * ks)) + 2048 * q);
        rd_tr8(wf[ks], qb + 16384 * jj + tro, 8192 * ks);
      }
      if (c < 8) xw_issue(c + 1, ldsb + ((c & 1) ? QB1 : QB2), h);
      kv_issue(e + 3); ++e;
#pragma unroll
      for (int ks = 0; ks < 2; ++ks) {
        if (c < 8) WAITV(19); else WAITV(8);
        BARRIER();
        kv_issue(e + 3); ++e;
        mm24(wf[ks], xf[ks]);
      }
    }
    WAITV(8); BARRIER();
    kv_issue(33);
    pack_q();
    WAITV(8); BARRIER();
    kv_issue(34);
  }

  const unsigned prot = (b >> 3) + (b & 1), ksrot = b & 7;
  unsigned vo[3];
#pragma unroll
  for (int i = 0; i < 3; ++i) { const unsigned L = 64 * i + lane, row = L / 24, ph = L - 24 * row; vo[i] = row * (DM * 2) + 16 * (ph ^ (row & 6)) - 1024 * i; }
  auto wo_issue = [&](unsigned i, unsigned buf) {
    const unsigned r = i >= 24, k = i - 24 * r; unsigned p = (k >> 3) + prot; if (p >= 3) p -= 3;
    dma3(ro, buf + 3072 * u, vo[0], vo[1], vo[2], ((hb + 2 * r) * DH + 32 * ((k + ksrot) & 7) + 8 * u) * (DM * 2) + 384 * p);
  };

  {
    zero_acc();
    f32x4 negm[3] = {f32x4{}, f32x4{}, f32x4{}};
    float l[3] = {0.f, 0.f, 0.f};
    const unsigned koff = ldsb + KVS + 8192 * jj + 256 * (8 * q4 + p4) + 16 * (g ^ c16), voff = ldsb + 8192 * jj + tro;
    f32x4 sc[2][3];
    bf16x8 kf[2][4];
    auto qk = [&]() {
#pragma unroll
      for (int kb = 0; kb < 2; ++kb)
#pragma unroll
        for (int q = 0; q < 3; ++q) sc[kb][q] = MFMA16(kf[kb][0], qf[0][q], negm[q]);
#pragma unroll
      for (int s = 1; s < 4; ++s)
#pragma unroll
        for (int kb = 0; kb < 2; ++kb)
#pragma unroll
          for (int q = 0; q < 3; ++q) sc[kb][q] = MFMA16(kf[kb][s], qf[s][q], sc[kb][q]);
    };
    WAITV(4);
    BARRIER();
#pragma unroll
    for (int s = 0; s < 4; ++s) { const unsigned a = (2 * KVS + koff) ^ (64 * s); kf[0][s] = lds_b128(a); kf[1][s] = lds_b128(a + 1024); }
    qk();
#pragma clang loop unroll(disable)
    for (int t = 0; t < 16; ++t) {
      const unsigned sb = (t & 1) * (2 * KVS);
      BARRIER();
      bf16x8 vf[8];
      rd_tr8(vf, sb + voff, 0);
      MEMFENCE(); SB0();
      float mx[3];
#pragma unroll
      for (int q = 0; q < 3; ++q)
        mx[q] = max3f(max3f(sc[0][q][0], sc[0][q][1], sc[0][q][2]), max3f(sc[0][q][3], sc[1][q][0], sc[1][q][1]), max2f(sc[1][q][2], sc[1][q][3]));
      if (t == 0 || __builtin_expect(__any(!(max3f(mx[0], mx[1], mx[2]) <= THR)), 0)) {
#pragma unroll
        for (int q = 0; q < 3; ++q) {
          float dl = xg_max(mx[q]);
          if (t != 0) dl = max2f(dl, 0.f);
          const float al = t == 0 ? 0.f : __builtin_amdgcn_exp2f(-dl);
          negm[q] -= dl; sc[0][q] -= dl; sc[1][q] -= dl; l[q] *= al;
#pragma unroll
          for (int d = 0; d < 8; ++d) acc[d][q] *= al;
        }
      }
      bf16x8 pf[3];
#pragma unroll
      for (int q = 0; q < 3; ++q) {
#pragma unroll
        for (int i = 0; i < 4; ++i) { sc[0][q][i] = __builtin_amdgcn_exp2f(sc[0][q][i]); sc[1][q][i] = __builtin_amdgcn_exp2f(sc[1][q][i]); }
        l[q] += ((sc[0][q][0] + sc[0][q][1]) + (sc[0][q][2] + sc[0][q][3])) + ((sc[1][q][0] + sc[1][q][1]) + (sc[1][q][2] + sc[1][q][3]));
        pf[q] = pack2(sc[0][q], sc[1][q]);
      }
      asm volatile("" : "+v"(pf[0]), "+v"(pf[1]), "+v"(pf[2]), "+v"(l[0]), "+v"(l[1]), "+v"(l[2]));
      WAITV(0); BARRIER();
      if (t == 15 && team == 0) { wo_issue(0, ldsb + WREG); kv_issue(35); }
#pragma unroll
      for (int d = 0; d < 8; ++d) {
#pragma unroll
        for (int q = 0; q < 3; ++q) acc[d][q] = MFMA16(vf[d], pf[q], acc[d][q]);
        if (d >= 2 && d < 6) { const unsigned a = (sb + koff) ^ (64 * (d - 2)); kf[0][d - 2] = lds_b128(a); kf[1][d - 2] = lds_b128(a + 1024); MEMFENCE(); }
        SB0();
      }
      if (t < 15) qk();
    }
#pragma unroll
    for (int q = 0; q < 3; ++q) {
      const float inv = 1.0f / xg_sum(l[q]);
#pragma unroll
      for (int s = 0; s < 4; ++s)
        *(LAS bf16x8*)(uintptr_t)(ldsb + OREG + (((4 * jj + s - ksrot) & 7) * 6 + 3 * qh + q) * 1024 + lane * 16) = pack2(acc[2 * s][q] * inv, acc[2 * s + 1][q] * inv);
    }
  }

  if (team == 0) {
    unsigned wof[6];
#pragma unroll
    for (int n = 0; n < 6; ++n) wof[n] = ldsb + (4 * g + q4) * 384 + 16 * ((12 * jj + 2 * n + (p4 >> 1)) ^ ((4 * g + q4) & 6)) + 8 * (p4 & 1);
    const unsigned oof = ldsb + OREG + 3 * qh * 1024 + lane * 16;
    f32x4 oc[6][3];
    unsigned pA[36], pB[36], pC[36];
#pragma unroll
    for (int i = 0; i < 36; ++i) { pB[i] = 0; pC[i] = 0; }
    auto mm18 = [&](const bf16x8 (&w)[6], const bf16x8 (&o)[3]) {
#pragma unroll
      for (int n = 0; n < 6; ++n)
#pragma unroll
        for (int q = 0; q < 3; ++q) oc[n][q] = MFMA16(w[n], o[q], oc[n][q]);
    };
    auto r1_issue = [&](unsigned e) { wo_issue(e - 32, ldsb + WREG + (e & 3) * WOU); kv_issue(e + 3); };
    BARRIER(); r1_issue(33);
    BARRIER(); r1_issue(34);
    unsigned e = 35;
#pragma clang loop unroll(disable)
    for (int p = 0; p < 3; ++p) {
#pragma unroll
      for (int n = 0; n < 6; ++n)
#pragma unroll
        for (int q = 0; q < 3; ++q) oc[n][q] = f32x4{};
#pragma clang loop unroll(disable)
      for (unsigned k = 0; k < 8; ++k, ++e) {
        WAITV(14); BARRIER();
        bf16x8 of[3], wf[6];
        const unsigned wb = WREG + (k & 3) * WOU, ob = oof + k * 6144;
#pragma unroll
        for (int q = 0; q < 3; ++q) of[q] = lds_b128(ob + 1024 * q);
#pragma unroll
        for (int n = 0; n < 6; ++n) wf[n] = lds_tr2(wb + wof[n], wb + wof[n] + 16 * 384);
        r1_issue(e);
        mm18(wf, of);
      }
#pragma unroll
      for (int i = 0; i < 36; ++i) {
        const f32x4 v = oc[i / 6][(i / 2) % 3];
        pA[i] = pB[i]; pB[i] = pC[i]; pC[i] = cvtpk(v[2 * (i & 1)], v[2 * (i & 1) + 1]);
      }
    }
    WAITV(14); BARRIER(); r1_issue(59);
    WAITV(14); BARRIER(); kv_issue(63);
    auto unpark = [&](const unsigned (&p)[36]) {
#pragma unroll
      for (int i = 0; i < 18; ++i) {
        unsigned v0 = p[2 * i], v1 = p[2 * i + 1]; asm volatile("" : "+v"(v0), "+v"(v1));
        oc[i / 3][i % 3] = f32x4{__uint_as_float(v0 << 16), __uint_as_float(v0 & 0xffff0000u), __uint_as_float(v1 << 16), __uint_as_float(v1 & 0xffff0000u)};
      }
    };
    WAITV(11); BARRIER();
    unpark(pA);
    unsigned woh[6];
#pragma unroll
    for (int n = 0; n < 6; ++n) { woh[n] = wof[n] + WREG; asm volatile("" : "+v"(woh[n])); }
    bf16x8 wa[6], wb[6], oa[3], ob[3];
    WAITV(4); BARRIER();
    WAITV(0); BARRIER();
    wo_issue(28, ldsb); wo_issue(29, ldsb + WOU);
    BARRIER();
    wo_issue(30, ldsb + 2 * WOU); wo_issue(31, ldsb + 3 * WOU);
    const __amdgpu_buffer_rsrc_t rp = mkrsrc(ows + (size_t)((blk * NB + b) * 8 + (hb >> 2)) * PART, PART);
    auto store_pass = [&](unsigned pi) {
      unsigned p = pi + prot; if (p >= 3) p -= 3;
#pragma unroll
      for (int n = 0; n < 3; ++n)
#pragma unroll
        for (int q = 0; q < 3; ++q) __builtin_amdgcn_raw_buffer_store_b128(__builtin_bit_cast(u32x4, pack_swap(oc[2 * n][q], oc[2 * n + 1][q])), rp, lane * 16 + (n * 3 + q) * 1024, p * 36864 + u * 9216, STAUX);
    };
    auto rd_unit = [&](bf16x8 (&w)[6], bf16x8 (&o)[3], const int k) {
#pragma unroll
      for (int q = 0; q < 3; ++q) o[q] = lds_b128(oof + k * 6144 + 1024 * q);
#pragma unroll
      for (int n = 0; n < 6; ++n) { const unsigned a = (k < 4 ? woh[n] : wof[n]) + (k & 3) * WOU; w[n] = lds_tr2(a, a + 16 * 384); }
    };
    BARRIER();
    rd_unit(wa, oa, 0);
#pragma clang loop unroll(disable)
    for (unsigned p = 0; p < 3; ++p) {
#pragma unroll
      for (int j = 0; j < 4; ++j) {
        rd_unit(wb, ob, 2 * j + 1);
        mm18(wa, oa);
        weave<15>();
        if (j == 1 && p == 0) WAITV(0);
        BARRIER();
        rd_unit(wa, oa, (2 * j + 2) & 7);
        mm18(wb, ob);
        weave<15>();
        MEMFENCE(); SB0();
      }
      store_pass(p);
      if (p == 0) unpark(pB); else unpark(pC);
    }
  } else {
#pragma clang loop unroll(disable)
    for (unsigned j = 0; j < 13; ++j) {
      if (j < 10) WAITV(12); else if (j == 10) WAITV(6); else WAITV(0);
      BARRIER();
      if (j >= 1 && j < 9) {
        const unsigned k = 2 * j + 6, buf = ldsb + ((k & 4) ? 0 : WREG) + (k & 3) * WOU;
        wo_issue(24 + k, buf); wo_issue(25 + k, buf + WOU);
      }
    }
  }
  asm volatile("s_waitcnt vmcnt(0)" ::: "memory");
}

__global__ __launch_bounds__(256) void reduce_out(const char* __restrict__ ows, char* __restrict__ out) {
  const unsigned t = blockIdx.x * 256 + threadIdx.x, grp = t / 6912, r = t - grp * 6912;
  const char* p = ows + (size_t)grp * 8 * PART + r * 16;
  u32x4 v[8];
#pragma unroll
  for (int i = 0; i < 8; ++i) v[i] = *(const u32x4*)(p + i * PART);
  float s[8] = {0.f, 0.f, 0.f, 0.f, 0.f, 0.f, 0.f, 0.f};
#pragma unroll
  for (int i = 0; i < 8; ++i)
#pragma unroll
    for (int k = 0; k < 4; ++k) { s[2 * k] += __uint_as_float(v[i][k] << 16); s[2 * k + 1] += __uint_as_float(v[i][k] & 0xffff0000u); }
  const unsigned lane = r & 63, f = r >> 6, pu = f / 9, i9 = f - 9 * pu, np = i9 / 3, q = i9 - 3 * np, p_ = pu >> 2, u = pu & 3;
  const unsigned row = 48 * (u & 1) + 16 * q + (lane & 15), col = 192 * p_ + 96 * (u >> 1) + 32 * np + 8 * (lane >> 4);
  u32x4 o = {cvtpk(s[0], s[1]), cvtpk(s[2], s[3]), cvtpk(s[4], s[5]), cvtpk(s[6], s[7])};
  *(u32x4*)(out + ((size_t)((grp & 15) * SQ + row) * (2 * DM) + (grp >> 4) * DM + col) * 2) = o;
}

extern "C" void kernel_launch(void* const* d_in, const int* in_sizes, int n_in, void* d_out, int out_size, void* d_ws, size_t ws_size,
                              hipStream_t stream) {
  (void)in_sizes; (void)n_in; (void)out_size; (void)ws_size;
  if (WSEND) d_ws = (char*)d_ws + ((ws_size - (size_t)256 * PART) & ~(size_t)4095);
  (void)hipFuncSetAttribute((const void*)attn_main, hipFuncAttributeMaxDynamicSharedMemorySize, LDS_MAIN);
  attn_main<<<256, 512, LDS_MAIN, stream>>>((const char*)d_in[0], (const char*)d_in[1], (const char*)d_in[2], (const char*)d_in[3], (const char*)d_in[4],
                                            (const char*)d_in[5], (const char*)d_in[6], (const char*)d_in[7], (const char*)d_in[8], (const char*)d_in[9], (char*)d_ws);
  reduce_out<<<864, 256, 0, stream>>>((const char*)d_ws, (char*)d_out);
}
```

```cpp
#include <hip/hip_runtime.h>
#include <hip/hip_bf16.h>
#include <stdint.h>

#define LAS __attribute__((address_space(3)))
#define DI __device__ __forceinline__
typedef __bf16 bf16x2 __attribute__((ext_vector_type(2)));
typedef __bf16 bf16x4 __attribute__((ext_vector_type(4)));
typedef __bf16 bf16x8 __attribute__((ext_vector_type(8)));
typedef float f32x2 __attribute__((ext_vector_type(2)));
typedef float f32x4 __attribute__((ext_vector_type(4)));
typedef unsigned u32x4 __attribute__((ext_vector_type(4)));
typedef unsigned u32x2 __attribute__((ext_vector_type(2)));

constexpr int NB = 16, SQ = 96, DM = 576, NH = 32, SK = 512, DH = 128, HD = NH * DH;
constexpr unsigned LDS_MAIN = 163840;
constexpr float SC_LOG2E = 0.08838834764831845f * 1.4426950408889634f;
constexpr float THR = 8.0f;

DI unsigned rfl(unsigned x) { return __builtin_amdgcn_readfirstlane(x); }
DI unsigned cvtpk(float lo, float hi) { f32x2 v = {lo, hi}; bf16x2 b = __builtin_convertvector(v, bf16x2); return __builtin_bit_cast(unsigned, b); }
DI bf16x8 pack2(const f32x4& a, const f32x4& b) {
  u32x4 w = {cvtpk(a[0], a[1]), cvtpk(a[2], a[3]), cvtpk(b[0], b[1]), cvtpk(b[2], b[3])};
  return __builtin_bit_cast(bf16x8, w);
}
DI bf16x8 pack_swap(const f32x4& a0, const f32x4& a1) {
  unsigned p00 = cvtpk(a0[0], a0[1]), p01 = cvtpk(a0[2], a0[3]), p10 = cvtpk(a1[0], a1[1]), p11 = cvtpk(a1[2], a1[3]);
  auto r0 = __builtin_amdgcn_permlane32_swap(p00, p10, false, false);
  auto r1 = __builtin_amdgcn_permlane32_swap(p01, p11, false, false);
  auto t0 = __builtin_amdgcn_permlane16_swap(r0[0], r0[1], false, false);
  auto t1 = __builtin_amdgcn_permlane16_swap(r1[0], r1[1], false, false);
  u32x4 w = {t0[0], t1[0], t0[1], t1[1]};
  return __builtin_bit_cast(bf16x8, w);
}
DI bf16x8 lds_b128(unsigned a) { return *(const LAS bf16x8*)(uintptr_t)a; }
DI bf16x8 lds_tr2(unsigned a0, unsigned a1) {
  bf16x4 lo = __builtin_amdgcn_ds_read_tr16_b64_v4bf16((LAS bf16x4*)(uintptr_t)a0);
  bf16x4 hi = __builtin_amdgcn_ds_read_tr16_b64_v4bf16((LAS bf16x4*)(uintptr_t)a1);
  return __builtin_shufflevector(lo, hi, 0, 1, 2, 3, 4, 5, 6, 7);
}
DI float max2f(float a, float b) { return __builtin_elementwise_maximum(a, b); }
DI float max3f(float a, float b, float c) { return max2f(max2f(a, b), c); }
DI float xg_max(float v) {
  auto r = __builtin_amdgcn_permlane32_swap(__float_as_uint(v), __float_as_uint(v), false, false); v = max2f(__uint_as_float(r[0]), __uint_as_float(r[1]));
  auto s = __builtin_amdgcn_permlane16_swap(__float_as_uint(v), __float_as_uint(v), false, false); return max2f(__uint_as_float(s[0]), __uint_as_float(s[1]));
}
DI float xg_sum(float v) {
  auto r = __builtin_amdgcn_permlane32_swap(__float_as_uint(v), __float_as_uint(v), false, false); v = __uint_as_float(r[0]) + __uint_as_float(r[1]);
  auto s = __builtin_amdgcn_permlane16_swap(__float_as_uint(v), __float_as_uint(v), false, false); return __uint_as_float(s[0]) + __uint_as_float(s[1]);
}
#define MFMA16(a, b, c) __builtin_amdgcn_mfma_f32_16x16x32_bf16((a), (b), (c), 0, 0, 0)
#define SB0() __builtin_amdgcn_sched_barrier(0)
#define MEMFENCE() asm volatile("" ::: "memory")
#define BARRIER() do { SB0(); __builtin_amdgcn_s_waitcnt(0xC07F); asm volatile("s_barrier" ::: "memory"); SB0(); } while (0)
#define WAITV(n) asm volatile("s_waitcnt vmcnt(" #n ")" ::: "memory")
template <int N> DI void weave() {
#pragma unroll
  for (int i = 0; i < N; ++i) { __builtin_amdgcn_sched_group_barrier(0x8, 1, 0); __builtin_amdgcn_sched_group_barrier(0x100, 1, 0); }
}
DI __amdgpu_buffer_rsrc_t mkrsrc(const void* p, unsigned bytes) { return __builtin_amdgcn_make_buffer_rsrc((void*)p, (short)0, (int)bytes, 0x00020000); }
DI void dma4_nt(__amdgpu_buffer_rsrc_t r, unsigned lds, unsigned va, unsigned vb, unsigned so) {
  asm volatile("s_mov_b32 m0, %0\n\ts_nop 0\n\t"
               "buffer_load_dwordx4 %1, %3, %4 offen nt lds\n\t"
               "buffer_load_dwordx4 %1, %3, %4 offen offset:1024 nt lds\n\t"
               "buffer_load_dwordx4 %2, %3, %4 offen offset:2048 nt lds\n\t"
               "buffer_load_dwordx4 %2, %3, %4 offen offset:3072 nt lds"
               :: "s"(lds), "v"(va), "v"(vb), "s"(r), "s"(so) : "memory");
}
DI void dma4(__amdgpu_buffer_rsrc_t r, unsigned lds, unsigned v0, unsigned v1, unsigned v2, unsigned v3, unsigned so) {
  asm volatile("s_mov_b32 m0, %0\n\ts_nop 0\n\t"
               "buffer_load_dwordx4 %1, %5, %6 offen lds\n\t"
               "buffer_load_dwordx4 %2, %5, %6 offen offset:1024 lds\n\t"
               "buffer_load_dwordx4 %3, %5, %6 offen offset:2048 lds\n\t"
               "buffer_load_dwordx4 %4, %5, %6 offen offset:3072 lds"
               :: "s"(lds), "v"(v0), "v"(v1), "v"(v2), "v"(v3), "s"(r), "s"(so) : "memory");
}
DI void dma3(__amdgpu_buffer_rsrc_t r, unsigned lds, unsigned v0, unsigned v1, unsigned v2, unsigned so) {
  asm volatile("s_mov_b32 m0, %0\n\ts_nop 0\n\t"
               "buffer_load_dwordx4 %1, %4, %5 offen lds\n\t"
               "buffer_load_dwordx4 %2, %4, %5 offen offset:1024 lds\n\t"
               "buffer_load_dwordx4 %3, %4, %5 offen offset:2048 lds"
               :: "s"(lds), "v"(v0), "v"(v1), "v"(v2), "s"(r), "s"(so) : "memory");
}
DI void dma3w(__amdgpu_buffer_rsrc_t r, unsigned lds, unsigned v0, unsigned v1, unsigned v2, unsigned so) {
  asm volatile("s_mov_b32 m0, %0\n\ts_nop 3\n\t"
               "buffer_load_dwordx4 %1, %4, %5 offen lds\n\t"
               "buffer_load_dwordx4 %2, %4, %5 offen offset:1024 lds\n\t"
               "buffer_load_dwordx4 %3, %4, %5 offen offset:2048 lds"
               :: "s"(lds), "v"(v0), "v"(v1), "v"(v2), "s"(r), "s"(so) : "memory");
}
DI void touch(__amdgpu_buffer_rsrc_t r, unsigned lds, unsigned v, unsigned so) {
  asm volatile("s_mov_b32 m0, %0\n\ts_nop 0\n\tbuffer_load_dword %1, %2, %3 offen lds" :: "s"(lds), "v"(v), "s"(r), "s"(so) : "memory");
}

constexpr unsigned KVS = 16384, NKV = 4;
constexpr unsigned QB1 = 73728, QB2 = 118784;
constexpr unsigned SCR = 65536;
constexpr unsigned OREG = 65536, WREG = 114688, WOU = 12288;
constexpr unsigned PART = 110592;

__global__ __launch_bounds__(512, 2) void attn_main(const char* xA, const char* kA, const char* vA, const char* wqA, const char* woA,
                                                    const char* xB, const char* kB, const char* vB, const char* wqB, const char* woB, char* ows) {
  extern __shared__ __attribute__((aligned(16))) char lds[];
  const unsigned ldsb = rfl((unsigned)(uintptr_t)(LAS char*)lds);
  const unsigned wid = rfl(threadIdx.x >> 6), lane = threadIdx.x & 63, team = wid >> 2, u = wid & 3, jj = u >> 1, qh = u & 1;
  const unsigned id = blockIdx.x, xcd = id & 7, idx = id >> 3, blk = xcd >> 2;
  const unsigned b = idx & 15, hb = 8 * (xcd & 3) + 4 * (idx >> 4), h = hb + 2 * team + jj;
  const char* kp = blk ? kB : kA; const char* vp = blk ? vB : vA; const char* wop = blk ? woB : woA;
  const __amdgpu_buffer_rsrc_t rk = mkrsrc(kp + (size_t)(b * NH + hb) * SK * DH * 2, 4 * SK * DH * 2);
  const __amdgpu_buffer_rsrc_t rv = mkrsrc(vp + (size_t)(b * NH + hb) * SK * DH * 2, 4 * SK * DH * 2);
  const __amdgpu_buffer_rsrc_t ro = mkrsrc(wop, HD * DM * 2);
  const unsigned g = lane >> 4, c16 = lane & 15, q4 = c16 >> 2, p4 = lane & 3;
  const unsigned vka = g * 256 + 16 * (c16 ^ ((8 * qh) | g)), vkb = g * 256 + 16 * (c16 ^ ((8 * qh + 4) | g));
  const unsigned vva = g * 256 + 16 * (c16 ^ (g << 2)), vvb = g * 256 + 16 * (c16 ^ ((g << 2) | 2));
  const unsigned krot = (id * 5 + (id >> 3) + jj * 7) & 15;
  const unsigned kvs0 = jj * (SK * DH * 2) + 4096 * qh, kvd0 = ldsb + 8192 * jj + 4096 * qh;
  auto kv_so = [&](unsigned T) { return kvs0 + ((T & 16) << 14) + (((T + krot) & 15) << 13); };
  auto k_issue = [&](unsigned T) { dma4_nt(rk, kvd0 + 3 * KVS - (T & 1) * (2 * KVS), vka, vkb, kv_so(T)); };
  auto v_issue = [&](unsigned T) { dma4_nt(rv, kvd0 + (T & 1) * (2 * KVS), vva, vvb, kv_so(T)); };
  const unsigned tro = 256 * (8 * g + q4) + 16 * ((q4 << 2) | ((g & 1) << 1) | (p4 >> 1)) + 8 * (p4 & 1);
  const unsigned xo0 = 32768 + (48 * qh + c16) * 128 + 16 * (g ^ ((c16 >> 1) & 7));
  auto rd_tr8 = [&](bf16x8 (&f)[8], unsigned a, unsigned imm) {
#pragma unroll
    for (int d = 0; d < 8; ++d) f[d] = lds_tr2((a ^ (32 * d)) + imm, (a ^ (32 * d)) + imm + 1024);
  };

  bf16x8 qf[4][3];
  f32x4 acc[8][3];
  auto zero_acc = [&]() {
#pragma unroll
    for (int d = 0; d < 8; ++d)
#pragma unroll
      for (int q = 0; q < 3; ++q) acc[d][q] = f32x4{};
  };
  auto mm24 = [&](const bf16x8 (&a)[8], const bf16x8 (&x)[3]) {
#pragma unroll
    for (int d = 0; d < 8; ++d)
#pragma unroll
      for (int q = 0; q < 3; ++q) acc[d][q] = MFMA16(a[d], x[q], acc[d][q]);
  };
  auto pack_q = [&]() {
#pragma unroll
    for (int s = 0; s < 4; ++s)
#pragma unroll
      for (int q = 0; q < 3; ++q) qf[s][q] = pack_swap(acc[2 * s][q] * SC_LOG2E, acc[2 * s + 1][q] * SC_LOG2E);
  };

  if (team == 0) {
    zero_acc();
    bf16x8 wa[8], wb[8], xa[3], xb[3];
#pragma unroll
    for (int d = 0; d < 8; ++d) wb[d] = bf16x8{};
#pragma unroll
    for (int q = 0; q < 3; ++q) xb[q] = bf16x8{};
    unsigned bufb = ldsb;
#pragma clang loop unroll(disable)
    for (int c = 0; c < 9; ++c) {
      const unsigned a = bufb + 16384 * jj + tro, xo = bufb + xo0;
      bufb = bufb == ldsb ? ldsb + QB1 : bufb == ldsb + QB1 ? ldsb + QB2 : ldsb;
      BARRIER();
#pragma unroll
      for (int q = 0; q < 3; ++q) xa[q] = lds_b128(xo + 2048 * q);
      rd_tr8(wa, a, 0);
      mm24(wb, xb);
      weave<19>();
      SB0();
#pragma unroll
      for (int q = 0; q < 3; ++q) xb[q] = lds_b128((xo ^ 64) + 2048 * q);
      rd_tr8(wb, a, 8192);
      mm24(wa, xa);
      weave<19>();
    }
    mm24(wb, xb);
    pack_q();
  } else {
    const char* xp = blk ? xB : xA; const char* wp = blk ? wqB : wqA;
    const __amdgpu_buffer_rsrc_t rx = mkrsrc(xp + (size_t)b * SQ * DM * 2, SQ * DM * 2);
    const __amdgpu_buffer_rsrc_t rw = mkrsrc(wp, DM * HD * 2);
    unsigned vw[4], vx[3];
#pragma unroll
    for (int pp = 0; pp < 4; ++pp) vw[pp] = (4 * pp + g) * (HD * 2) - 1024 * pp + 16 * (c16 ^ ((g << 2) | (pp & 2)));
#pragma unroll
    for (int i = 0; i < 3; ++i) vx[i] = (8 * i + (lane >> 3)) * (DM * 2) - 1024 * i + 16 * ((lane & 7) ^ ((4 * (3 * u + i) + g) & 7));
    const unsigned qrot = (b + 4 * (idx >> 4)) % 9;
    auto x_issue = [&](unsigned c, unsigned buf) { dma3(rx, buf + 32768 + 3072 * u, vx[0], vx[1], vx[2], c * 128 + 24 * u * (DM * 2)); };
    auto w_issue = [&](unsigned c, unsigned ks, unsigned buf, unsigned head) {
      dma4(rw, buf + 16384 * jj + 8192 * ks + 4096 * qh, vw[0], vw[1], vw[2], vw[3], (c * 64 + 32 * ks + 16 * qh) * (HD * 2) + head * 256);
    };
    auto nxt = [&](unsigned c) { return c == 8 ? 0u : c + 1; };
    unsigned cq = qrot;
    x_issue(cq, ldsb); w_issue(cq, 0, ldsb, h - 2); w_issue(cq, 1, ldsb, h - 2); cq = nxt(cq);
    x_issue(cq, ldsb + QB1); w_issue(cq, 0, ldsb + QB1, h - 2); w_issue(cq, 1, ldsb + QB1, h - 2); cq = nxt(cq);
    { unsigned c = 2 + (lane >= 24) + (lane >= 48) + qrot; if (c >= 9) c -= 9;
      touch(rx, ldsb + SCR, (24 * u + lane % 24) * (DM * 2) + c * 128, 0);
      c = 4 + qrot; if (c >= 9) c -= 9;
      touch(rx, ldsb + SCR, (24 * u + 16 + (lane & 7)) * (DM * 2) + c * 128, 0); }
    unsigned bufb = ldsb + QB2;
    WAITV(13);
#pragma clang loop unroll(disable)
    for (int c = 0; c < 7; ++c) {
      BARRIER();
      x_issue(cq, bufb); w_issue(cq, 0, bufb, h - 2); w_issue(cq, 1, bufb, h - 2); cq = nxt(cq);
      bufb = bufb == ldsb ? ldsb + QB1 : bufb == ldsb + QB1 ? ldsb + QB2 : ldsb;
      WAITV(11);
    }
    k_issue(0);
    BARRIER();
    v_issue(0);
    WAITV(8); BARRIER();
    k_issue(1); v_issue(1);
    zero_acc();
    WAITV(12); BARRIER();
    x_issue(cq, ldsb + QB1); w_issue(cq, 0, ldsb + QB1, h); w_issue(cq, 1, ldsb + QB1, h); cq = nxt(cq);
    WAITV(19); BARRIER();
    k_issue(2);
    unsigned T = 1;
#pragma clang loop unroll(disable)
    for (int c = 0; c < 9; ++c) {
      const unsigned qb = ldsb + ((c & 1) ? QB2 : QB1), qn = ldsb + ((c & 1) ? QB1 : QB2);
      const bool fast = c & 1;
      bf16x8 wf[2][8], xf[2][3];
      if (c == 0) WAITV(4); else WAITV(8);
      BARRIER();
#pragma unroll
      for (int q = 0; q < 3; ++q) xf[0][q] = lds_b128(qb + xo0 + 2048 * q);
      rd_tr8(wf[0], qb + 16384 * jj + tro, 0);
      if (!fast) {
        if (c < 8) { x_issue(cq, qn); w_issue(cq, 0, qn, h); }
        v_issue(T + 1);
        if (c < 8) WAITV(15); else WAITV(8);
        BARRIER();
        k_issue(T + 2); ++T;
        mm24(wf[0], xf[0]);
        if (c < 8) WAITV(15); else WAITV(8);
        BARRIER();
      } else { x_issue(cq, qn); w_issue(cq, 0, qn, h); }
#pragma unroll
      for (int q = 0; q < 3; ++q) xf[1][q] = lds_b128(((qb + xo0) ^ 64) + 2048 * q);
      rd_tr8(wf[1], qb + 16384 * jj + tro, 8192);
      if (c < 8) w_issue(cq, 1, qn, h);
      cq = nxt(cq);
      v_issue(T + 1);
      if (fast) WAITV(19); else if (c < 8) WAITV(12); else WAITV(8);
      BARRIER();
      k_issue(T + 2); ++T;
      if (fast) mm24(wf[0], xf[0]);
      mm24(wf[1], xf[1]);
    }
    WAITV(8); BARRIER();
    v_issue(16);
    pack_q();
    WAITV(8); BARRIER();
    k_issue(17);
  }

  const unsigned prot = (b >> 3) + (b & 1), ksrot = b & 7;
  unsigned vo[3];
#pragma unroll
  for (int i = 0; i < 3; ++i) { const unsigned L = 64 * i + lane, row = L / 24, ph = L - 24 * row; vo[i] = row * (DM * 2) + 16 * (ph ^ (row & 6)) - 1024 * i; }
  unsigned wo_tab;
  { const unsigned r = lane >= 24, k = lane - 24 * r; unsigned p = (k >> 3) + prot; if (p >= 3) p -= 3;
    wo_tab = ((hb + 2 * r) * DH + 32 * ((k + ksrot) & 7) + 8 * u) * (DM * 2) + 384 * p; }
  auto wo_issue = [&](unsigned i, unsigned buf) { dma3w(ro, buf + 3072 * u, vo[0], vo[1], vo[2], __builtin_amdgcn_readlane(wo_tab, i)); };

  {
    zero_acc();
    f32x4 negm[3] = {f32x4{}, f32x4{}, f32x4{}};
    float l[3] = {0.f, 0.f, 0.f};
    const unsigned koff = ldsb + KVS + 8192 * jj + 256 * (8 * q4 + p4) + 16 * (g ^ c16), voff = ldsb + 8192 * jj + tro;
    f32x4 sc[2][3];
    bf16x8 kf[2][4];
    auto qk = [&]() {
#pragma unroll
      for (int kb = 0; kb < 2; ++kb)
#pragma unroll
        for (int q = 0; q < 3; ++q) sc[kb][q] = MFMA16(kf[kb][0], qf[0][q], negm[q]);
#pragma unroll
      for (int s = 1; s < 4; ++s)
#pragma unroll
        for (int kb = 0; kb < 2; ++kb)
#pragma unroll
          for (int q = 0; q < 3; ++q) sc[kb][q] = MFMA16(kf[kb][s], qf[s][q], sc[kb][q]);
    };
    WAITV(4);
    BARRIER();
#pragma unroll
    for (int s = 0; s < 4; ++s) { const unsigned a = (2 * KVS + koff) ^ (64 * s); kf[0][s] = lds_b128(a); kf[1][s] = lds_b128(a + 1024); }
    qk();
#pragma clang loop unroll(disable)
    for (int t = 0; t < 16; ++t) {
      const unsigned sb = (t & 1) * (2 * KVS);
      BARRIER();
      bf16x8 vf[8];
      rd_tr8(vf, sb + voff, 0);
      MEMFENCE(); SB0();
      float mx[3];
#pragma unroll
      for (int q = 0; q < 3; ++q)
        mx[q] = max3f(max3f(sc[0][q][0], sc[0][q][1], sc[0][q][2]), max3f(sc[0][q][3], sc[1][q][0], sc[1][q][1]), max2f(sc[1][q][2], sc[1][q][3]));
      if (t == 0 || __builtin_expect(__any(!(max3f(mx[0], mx[1], mx[2]) <= THR)), 0)) {
#pragma unroll
        for (int q = 0; q < 3; ++q) {
          float dl = xg_max(mx[q]);
          if (t != 0) dl = max2f(dl, 0.f);
          const float al = t == 0 ? 0.f : __builtin_amdgcn_exp2f(-dl);
          negm[q] -= dl; sc[0][q] -= dl; sc[1][q] -= dl; l[q] *= al;
#pragma unroll
          for (int d = 0; d < 8; ++d) acc[d][q] *= al;
        }
      }
      bf16x8 pf[3];
#pragma unroll
      for (int q = 0; q < 3; ++q) {
#pragma unroll
        for (int i = 0; i < 4; ++i) { sc[0][q][i] = __builtin_amdgcn_exp2f(sc[0][q][i]); sc[1][q][i] = __builtin_amdgcn_exp2f(sc[1][q][i]); }
        l[q] += ((sc[0][q][0] + sc[0][q][1]) + (sc[0][q][2] + sc[0][q][3])) + ((sc[1][q][0] + sc[1][q][1]) + (sc[1][q][2] + sc[1][q][3]));
        pf[q] = pack2(sc[0][q], sc[1][q]);
      }
      asm volatile("" : "+v"(pf[0]), "+v"(pf[1]), "+v"(pf[2]), "+v"(l[0]), "+v"(l[1]), "+v"(l[2]));
      WAITV(0); BARRIER();
      if (t == 15 && team == 0) { wo_issue(0, ldsb + WREG); wo_issue(1, ldsb + WREG + WOU); v_issue(17); }
#pragma unroll
      for (int d = 0; d < 8; ++d) {
#pragma unroll
        for (int q = 0; q < 3; ++q) acc[d][q] = MFMA16(vf[d], pf[q], acc[d][q]);
        if (d >= 2 && d < 6) { const unsigned a = (sb + koff) ^ (64 * (d - 2)); kf[0][d - 2] = lds_b128(a); kf[1][d - 2] = lds_b128(a + 1024); MEMFENCE(); }
        SB0();
      }
      if (t < 15) qk();
    }
#pragma unroll
    for (int q = 0; q < 3; ++q) {
      const float inv = 1.0f / xg_sum(l[q]);
#pragma unroll
      for (int s = 0; s < 4; ++s)
        *(LAS bf16x8*)(uintptr_t)(ldsb + OREG + (((4 * jj + s - ksrot) & 7) * 6 + 3 * qh + q) * 1024 + lane * 16) = pack2(acc[2 * s][q] * inv, acc[2 * s + 1][q] * inv);
    }
  }

  if (team == 0) {
    unsigned wof[6];
#pragma unroll
    for (int n = 0; n < 6; ++n) wof[n] = ldsb + (4 * g + q4) * 384 + 16 * ((12 * jj + 2 * n + (p4 >> 1)) ^ ((4 * g + q4) & 6)) + 8 * (p4 & 1);
    const unsigned oof = ldsb + OREG + 3 * qh * 1024 + lane * 16;
    f32x4 oc[6][3];
    unsigned pA[36], pB[36], pC[36];
#pragma unroll
    for (int i = 0; i < 36; ++i) { pB[i] = 0; pC[i] = 0; }
    auto mm18 = [&](const bf16x8 (&w)[6], const bf16x8 (&o)[3]) {
#pragma unroll
      for (int n = 0; n < 6; ++n)
#pragma unroll
        for (int q = 0; q < 3; ++q) oc[n][q] = MFMA16(w[n], o[q], oc[n][q]);
    };
    BARRIER(); wo_issue(2, ldsb + WREG + 2 * WOU); k_issue(18);
    unsigned T = 17;
#pragma clang loop unroll(disable)
    for (int p = 0; p < 3; ++p) {
#pragma unroll
      for (int n = 0; n < 6; ++n)
#pragma unroll
        for (int q = 0; q < 3; ++q) oc[n][q] = f32x4{};
#pragma clang loop unroll(disable)
      for (unsigned k = 0; k < 8; k += 2, ++T) {
        WAITV(11); BARRIER();
        bf16x8 of[2][3], wf[2][6];
        const unsigned wb = WREG + (k & 2) * WOU, ob = oof + k * 6144;
#pragma unroll
        for (int i = 0; i < 2; ++i) {
#pragma unroll
          for (int q = 0; q < 3; ++q) of[i][q] = lds_b128(ob + 6144 * i + 1024 * q);
#pragma unroll
          for (int n = 0; n < 6; ++n) wf[i][n] = lds_tr2(wb + wof[n] + WOU * i, wb + wof[n] + WOU * i + 16 * 384);
        }
        wo_issue(2 * T - 31, ldsb + WREG + ((k & 2) ^ 3) * WOU);
        v_issue(T + 1);
        WAITV(14); BARRIER();
        wo_issue(2 * T - 30, ldsb + wb);
        k_issue(T + 2);
        mm18(wf[0], of[0]); mm18(wf[1], of[1]);
      }
#pragma unroll
      for (int i = 0; i < 36; ++i) {
        const f32x4 v = oc[i / 6][(i / 2) % 3];
        pA[i] = pB[i]; pB[i] = pC[i]; pC[i] = cvtpk(v[2 * (i & 1)], v[2 * (i & 1) + 1]);
      }
    }
    WAITV(14); BARRIER(); wo_issue(27, ldsb + WREG + 3 * WOU); v_issue(30);
    WAITV(14); BARRIER(); k_issue(31);
    WAITV(11); BARRIER(); v_issue(31);
    auto unpark = [&](const unsigned (&p)[36]) {
#pragma unroll
      for (int i = 0; i < 18; ++i) {
        unsigned v0 = p[2 * i], v1 = p[2 * i + 1]; asm volatile("" : "+v"(v0), "+v"(v1));
        oc[i / 3][i % 3] = f32x4{__uint_as_float(v0 << 16), __uint_as_float(v0 & 0xffff0000u), __uint_as_float(v1 << 16), __uint_as_float(v1 & 0xffff0000u)};
      }
    };
    WAITV(8); BARRIER();
    unpark(pA);
    unsigned woh[6];
#pragma unroll
    for (int n = 0; n < 6; ++n) { woh[n] = wof[n] + WREG; asm volatile("" : "+v"(woh[n])); }
    bf16x8 wa[6], wb[6], oa[3], ob[3];
    WAITV(4); BARRIER();
    WAITV(0); BARRIER();
    wo_issue(28, ldsb); wo_issue(29, ldsb + WOU);
    BARRIER();
    wo_issue(30, ldsb + 2 * WOU); wo_issue(31, ldsb + 3 * WOU);
    const __amdgpu_buffer_rsrc_t rp = mkrsrc(ows + (size_t)((blk * NB + b) * 8 + (hb >> 2)) * PART, PART);
    auto store_pass = [&](unsigned pi) {
      unsigned p = pi + prot; if (p >= 3) p -= 3;
#pragma unroll
      for (int n = 0; n < 3; ++n)
#pragma unroll
        for (int q = 0; q < 3; ++q) __builtin_amdgcn_raw_buffer_store_b128(__builtin_bit_cast(u32x4, pack_swap(oc[2 * n][q], oc[2 * n + 1][q])), rp, lane * 16 + (n * 3 + q) * 1024, p * 36864 + u * 9216, 16);
    };
    auto rd_unit = [&](bf16x8 (&w)[6], bf16x8 (&o)[3], const int k) {
#pragma unroll
      for (int q = 0; q < 3; ++q) o[q] = lds_b128(oof + k * 6144 + 1024 * q);
#pragma unroll
      for (int n = 0; n < 6; ++n) { const unsigned a = (k < 4 ? woh[n] : wof[n]) + (k & 3) * WOU; w[n] = lds_tr2(a, a + 16 * 384); }
    };
    BARRIER();
    rd_unit(wa, oa, 0);
#pragma clang loop unroll(disable)
    for (unsigned p = 0; p < 3; ++p) {
#pragma unroll
      for (int j = 0; j < 4; ++j) {
        rd_unit(wb, ob, 2 * j + 1);
        mm18(wa, oa);
        weave<15>();
        if (j == 1 && p == 0) WAITV(0);
        BARRIER();
        rd_unit(wa, oa, (2 * j + 2) & 7);
        mm18(wb, ob);
        weave<15>();
        MEMFENCE(); SB0();
      }
      store_pass(p);
      if (p == 0) unpark(pB); else unpark(pC);
    }
  } else {
#pragma clang loop unroll(disable)
    for (unsigned j = 0; j < 13; ++j) {
      if (j < 10) WAITV(12); else if (j == 10) WAITV(6); else WAITV(0);
      BARRIER();
      if (j >= 1 && j < 9) {
        const unsigned k = 2 * j + 6, buf = ldsb + ((k & 4) ? 0 : WREG) + (k & 3) * WOU;
        wo_issue(24 + k, buf); wo_issue(25 + k, buf + WOU);
      }
    }
  }
  asm volatile("s_waitcnt vmcnt(0)" ::: "memory");
}

__global__ __launch_bounds__(256) void reduce_out(const char* __restrict__ ows, char* __restrict__ out) {
  const unsigned t = blockIdx.x * 256 + threadIdx.x, grp = t / 6912, r = t - grp * 6912;
  const char* p = ows + (size_t)grp * 8 * PART + r * 16;
  u32x4 v[8];
#pragma unroll
  for (int i = 0; i < 8; ++i) v[i] = *(const u32x4*)(p + i * PART);
  float s[8] = {0.f, 0.f, 0.f, 0.f, 0.f, 0.f, 0.f, 0.f};
#pragma unroll
  for (int i = 0; i < 8; ++i)
#pragma unroll
    for (int k = 0; k < 4; ++k) { s[2 * k] += __uint_as_float(v[i][k] << 16); s[2 * k + 1] += __uint_as_float(v[i][k] & 0xffff0000u); }
  const unsigned lane = r & 63, f = r >> 6, pu = f / 9, i9 = f - 9 * pu, np = i9 / 3, q = i9 - 3 * np, p_ = pu >> 2, u = pu & 3;
  const unsigned row = 48 * (u & 1) + 16 * q + (lane & 15), col = 192 * p_ + 96 * (u >> 1) + 32 * np + 8 * (lane >> 4);
  u32x4 o = {cvtpk(s[0], s[1]), cvtpk(s[2], s[3]), cvtpk(s[4], s[5]), cvtpk(s[6], s[7])};
  *(u32x4*)(out + ((size_t)((grp & 15) * SQ + row) * (2 * DM) + (grp >> 4) * DM + col) * 2) = o;
}

extern "C" void kernel_launch(void* const* d_in, const int* in_sizes, int n_in, void* d_out, int out_size, void* d_ws, size_t ws_size,
                              hipStream_t stream) {
  (void)in_sizes; (void)n_in; (void)out_size; (void)ws_size;
  (void)hipFuncSetAttribute((const void*)attn_main, hipFuncAttributeMaxDynamicSharedMemorySize, LDS_MAIN);
  attn_main<<<256, 512, LDS_MAIN, stream>>>((const char*)d_in[0], (const char*)d_in[1], (const char*)d_in[2], (const char*)d_in[3], (const char*)d_in[4],
                                            (const char*)d_in[5], (const char*)d_in[6], (const char*)d_in[7], (const char*)d_in[8], (const char*)d_in[9], (char*)d_ws);
  reduce_out<<<864, 256, 0, stream>>>((const char*)d_ws, (char*)d_out);
}
```

```cpp
#include <hip/hip_runtime.h>
#include <hip/hip_bf16.h>
#include <stdint.h>
#include <type_traits>

#define LAS __attribute__((address_space(3)))
#define DI __device__ __forceinline__
typedef __bf16 bf16x2 __attribute__((ext_vector_type(2)));
typedef __bf16 bf16x4 __attribute__((ext_vector_type(4)));
typedef __bf16 bf16x8 __attribute__((ext_vector_type(8)));
typedef float f32x2 __attribute__((ext_vector_type(2)));
typedef float f32x4 __attribute__((ext_vector_type(4)));
typedef unsigned u32x4 __attribute__((ext_vector_type(4)));
typedef unsigned u32x2 __attribute__((ext_vector_type(2)));

constexpr int NB = 16, SQ = 96, DM = 576, NH = 32, SK = 512, DH = 128, HD = NH * DH;
constexpr unsigned LDS_MAIN = 163840;
constexpr float SC_LOG2E = 0.08838834764831845f * 1.4426950408889634f;
constexpr float THR = 8.0f;

DI unsigned rfl(unsigned x) { return __builtin_amdgcn_readfirstlane(x); }
DI unsigned cvtpk(float lo, float hi) { f32x2 v = {lo, hi}; bf16x2 b = __builtin_convertvector(v, bf16x2); return __builtin_bit_cast(unsigned, b); }
DI bf16x8 pack2(const f32x4& a, const f32x4& b) {
  u32x4 w = {cvtpk(a[0], a[1]), cvtpk(a[2], a[3]), cvtpk(b[0], b[1]), cvtpk(b[2], b[3])};
  return __builtin_bit_cast(bf16x8, w);
}
DI bf16x8 pack_swap(const f32x4& a0, const f32x4& a1) {
  unsigned p00 = cvtpk(a0[0], a0[1]), p01 = cvtpk(a0[2], a0[3]), p10 = cvtpk(a1[0], a1[1]), p11 = cvtpk(a1[2], a1[3]);
  auto r0 = __builtin_amdgcn_permlane32_swap(p00, p10, false, false);
  auto r1 = __builtin_amdgcn_permlane32_swap(p01, p11, false, false);
  auto t0 = __builtin_amdgcn_permlane16_swap(r0[0], r0[1], false, false);
  auto t1 = __builtin_amdgcn_permlane16_swap(r1[0], r1[1], false, false);
  u32x4 w = {t0[0], t1[0], t0[1], t1[1]};
  return __builtin_bit_cast(bf16x8, w);
}
DI bf16x8 lds_b128(unsigned a) { return *(const LAS bf16x8*)(uintptr_t)a; }
DI bf16x8 lds_tr2(unsigned a0, unsigned a1) {
  bf16x4 lo = __builtin_amdgcn_ds_read_tr16_b64_v4bf16((LAS bf16x4*)(uintptr_t)a0);
  bf16x4 hi = __builtin_amdgcn_ds_read_tr16_b64_v4bf16((LAS bf16x4*)(uintptr_t)a1);
  return __builtin_shufflevector(lo, hi, 0, 1, 2, 3, 4, 5, 6, 7);
}
DI float max2f(float a, float b) { return __builtin_elementwise_maximum(a, b); }
DI float max3f(float a, float b, float c) { return max2f(max2f(a, b), c); }
DI float xg_max(float v) {
  auto r = __builtin_amdgcn_permlane32_swap(__float_as_uint(v), __float_as_uint(v), false, false); v = max2f(__uint_as_float(r[0]), __uint_as_float(r[1]));
  auto s = __builtin_amdgcn_permlane16_swap(__float_as_uint(v), __float_as_uint(v), false, false); return max2f(__uint_as_float(s[0]), __uint_as_float(s[1]));
}
DI float xg_sum(float v) {
  auto r = __builtin_amdgcn_permlane32_swap(__float_as_uint(v), __float_as_uint(v), false, false); v = __uint_as_float(r[0]) + __uint_as_float(r[1]);
  auto s = __builtin_amdgcn_permlane16_swap(__float_as_uint(v), __float_as_uint(v), false, false); return __uint_as_float(s[0]) + __uint_as_float(s[1]);
}
#define MFMA16(a, b, c) __builtin_amdgcn_mfma_f32_16x16x32_bf16((a), (b), (c), 0, 0, 0)
#define SB0() __builtin_amdgcn_sched_barrier(0)
#define MEMFENCE() asm volatile("" ::: "memory")
#define BARRIER() do { SB0(); __builtin_amdgcn_s_waitcnt(0xC07F); asm volatile("s_barrier" ::: "memory"); SB0(); } while (0)
#define WAITV(n) asm volatile("s_waitcnt vmcnt(" #n ")" ::: "memory")
template <int N> DI void weave() {
#pragma unroll
  for (int i = 0; i < N; ++i) { __builtin_amdgcn_sched_group_barrier(0x8, 1, 0); __builtin_amdgcn_sched_group_barrier(0x100, 1, 0); }
}
DI __amdgpu_buffer_rsrc_t mkrsrc(const void* p, unsigned bytes) { return __builtin_amdgcn_make_buffer_rsrc((void*)p, (short)0, (int)bytes, 0x00020000); }
DI void dma4_nt(__amdgpu_buffer_rsrc_t r, unsigned lds, unsigned va, unsigned vb, unsigned so) {
  asm volatile("s_mov_b32 m0, %0\n\ts_nop 0\n\t"
               "buffer_load_dwordx4 %1, %3, %4 offen nt lds\n\t"
               "buffer_load_dwordx4 %1, %3, %4 offen offset:1024 nt lds\n\t"
               "buffer_load_dwordx4 %2, %3, %4 offen offset:2048 nt lds\n\t"
               "buffer_load_dwordx4 %2, %3, %4 offen offset:3072 nt lds"
               :: "s"(lds), "v"(va), "v"(vb), "s"(r), "s"(so) : "memory");
}
DI void dma4(__amdgpu_buffer_rsrc_t r, unsigned lds, unsigned v0, unsigned v1, unsigned v2, unsigned v3, unsigned so) {
  asm volatile("s_mov_b32 m0, %0\n\ts_nop 0\n\t"
               "buffer_load_dwordx4 %1, %5, %6 offen lds\n\t"
               "buffer_load_dwordx4 %2, %5, %6 offen offset:1024 lds\n\t"
               "buffer_load_dwordx4 %3, %5, %6 offen offset:2048 lds\n\t"
               "buffer_load_dwordx4 %4, %5, %6 offen offset:3072 lds"
               :: "s"(lds), "v"(v0), "v"(v1), "v"(v2), "v"(v3), "s"(r), "s"(so) : "memory");
}
DI void dma3(__amdgpu_buffer_rsrc_t r, unsigned lds, unsigned v0, unsigned v1, unsigned v2, unsigned so) {
  asm volatile("s_mov_b32 m0, %0\n\ts_nop 0\n\t"
               "buffer_load_dwordx4 %1, %4, %5 offen lds\n\t"
               "buffer_load_dwordx4 %2, %4, %5 offen offset:1024 lds\n\t"
               "buffer_load_dwordx4 %3, %4, %5 offen offset:2048 lds"
               :: "s"(lds), "v"(v0), "v"(v1), "v"(v2), "s"(r), "s"(so) : "memory");
}
DI void dma3w(__amdgpu_buffer_rsrc_t r, unsigned lds, unsigned v0, unsigned v1, unsigned v2, unsigned so) {
  asm volatile("s_mov_b32 m0, %0\n\ts_nop 3\n\t"
               "buffer_load_dwordx4 %1, %4, %5 offen lds\n\t"
               "buffer_load_dwordx4 %2, %4, %5 offen offset:1024 lds\n\t"
               "buffer_load_dwordx4 %3, %4, %5 offen offset:2048 lds"
               :: "s"(lds), "v"(v0), "v"(v1), "v"(v2), "s"(r), "s"(so) : "memory");
}
DI void touch(__amdgpu_buffer_rsrc_t r, unsigned lds, unsigned v, unsigned so) {
  asm volatile("s_mov_b32 m0, %0\n\ts_nop 0\n\tbuffer_load_dword %1, %2, %3 offen lds" :: "s"(lds), "v"(v), "s"(r), "s"(so) : "memory");
}

#define CODEPF 1
#define TLBPF 1
constexpr unsigned KVS = 16384, NKV = 4;
constexpr unsigned QB1 = 73728, QB2 = 118784;
constexpr unsigned SCR = 65536;
constexpr unsigned OREG = 65536, WREG = 114688, WOU = 12288;
constexpr unsigned FLG = 2048;
constexpr unsigned PART = 110592;

__global__ __launch_bounds__(512, 2) void attn_main(const char* xA, const char* kA, const char* vA, const char* wqA, const char* woA,
                                                    const char* xB, const char* kB, const char* vB, const char* wqB, const char* woB, char* ows, char* out) {
  extern __shared__ __attribute__((aligned(16))) char lds[];
  const unsigned ldsb = rfl((unsigned)(uintptr_t)(LAS char*)lds);
  const unsigned wid = rfl(threadIdx.x >> 6), lane = threadIdx.x & 63, team = wid >> 2, u = wid & 3, jj = u >> 1, qh = u & 1;
  const unsigned id = blockIdx.x, xcd = id & 7, idx = id >> 3, blk = xcd >> 2;
  const unsigned b = idx & 15, hb = 8 * (xcd & 3) + 4 * (idx >> 4), h = hb + 2 * team + jj;
  const char* kp = blk ? kB : kA; const char* vp = blk ? vB : vA; const char* wop = blk ? woB : woA;
  const __amdgpu_buffer_rsrc_t rk = mkrsrc(kp + (size_t)(b * NH + hb) * SK * DH * 2, 4 * SK * DH * 2);
  const __amdgpu_buffer_rsrc_t rv = mkrsrc(vp + (size_t)(b * NH + hb) * SK * DH * 2, 4 * SK * DH * 2);
  const __amdgpu_buffer_rsrc_t ro = mkrsrc(wop, HD * DM * 2);
  const unsigned grp = blk * NB + b, mem = hb >> 2;
  const __amdgpu_buffer_rsrc_t rf = mkrsrc(ows + (size_t)256 * PART + grp * FLG, FLG);
  const unsigned g = lane >> 4, c16 = lane & 15, q4 = c16 >> 2, p4 = lane & 3;
  const unsigned vka = g * 256 + 16 * (c16 ^ ((8 * qh) | g)), vkb = g * 256 + 16 * (c16 ^ ((8 * qh + 4) | g));
  const unsigned vva = g * 256 + 16 * (c16 ^ (g << 2)), vvb = g * 256 + 16 * (c16 ^ ((g << 2) | 2));
  const unsigned krot = (id * 5 + (id >> 3) + jj * 7) & 15;
  const unsigned kvs0 = jj * (SK * DH * 2) + 4096 * qh, kvd0 = ldsb + 8192 * jj + 4096 * qh;
  auto kv_so = [&](unsigned T) { return kvs0 + ((T & 16) << 14) + (((T + krot) & 15) << 13); };
  auto k_issue = [&](unsigned T) { dma4_nt(rk, kvd0 + 3 * KVS - (T & 1) * (2 * KVS), vka, vkb, kv_so(T)); };
  auto v_issue = [&](unsigned T) { dma4_nt(rv, kvd0 + (T & 1) * (2 * KVS), vva, vvb, kv_so(T)); };
  const unsigned tro = 256 * (8 * g + q4) + 16 * ((q4 << 2) | ((g & 1) << 1) | (p4 >> 1)) + 8 * (p4 & 1);
  const unsigned xo0 = 32768 + (48 * qh + c16) * 128 + 16 * (g ^ ((c16 >> 1) & 7));
  auto rd_tr8 = [&](bf16x8 (&f)[8], unsigned a, unsigned imm) {
#pragma unroll
    for (int d = 0; d < 8; ++d) f[d] = lds_tr2((a ^ (32 * d)) + imm, (a ^ (32 * d)) + imm + 1024);
  };

  bf16x8 qf[4][3];
  f32x4 acc[8][3];
  auto zero_acc = [&]() {
#pragma unroll
    for (int d = 0; d < 8; ++d)
#pragma unroll
      for (int q = 0; q < 3; ++q) acc[d][q] = f32x4{};
  };
  auto mm24 = [&](const bf16x8 (&a)[8], const bf16x8 (&x)[3]) {
#pragma unroll
    for (int d = 0; d < 8; ++d)
#pragma unroll
      for (int q = 0; q < 3; ++q) acc[d][q] = MFMA16(a[d], x[q], acc[d][q]);
  };
  auto pack_q = [&]() {
#pragma unroll
    for (int s = 0; s < 4; ++s)
#pragma unroll
      for (int q = 0; q < 3; ++q) qf[s][q] = pack_swap(acc[2 * s][q] * SC_LOG2E, acc[2 * s + 1][q] * SC_LOG2E);
  };

  unsigned long long pc0; unsigned clen;
  asm volatile("attn_code_begin:\n\ts_getpc_b64 %0\n\ts_mov_b32 %1, attn_code_end-attn_code_begin-8" : "=s"(pc0), "=s"(clen));
  if (team == 0) {
    if (u == 1) touch(mkrsrc((const void*)pc0, clen), ldsb + SCR + 256, (idx * 8 + (lane & 7)) * 128, 0);
    if (u == 2) { touch(rk, ldsb + SCR + 512, (lane & 3) * 131072, 0); touch(rv, ldsb + SCR + 512, (lane & 3) * 131072, 0); }
    if (u == 3) { touch(ro, ldsb + SCR + 512, 0, hb * DH * (DM * 2)); touch(mkrsrc(ows + (size_t)grp * 8 * PART, 8 * PART), ldsb + SCR + 512, (lane & 7) * PART, 0); touch(mkrsrc(out + (size_t)b * SQ * 2 * DM * 2, 4096), ldsb + SCR + 512, 0, 0); }
    if (u == 0) {
      const unsigned old = __builtin_amdgcn_raw_buffer_load_b32(rf, lane < 24 ? 128 * (1 + mem) + 4 * lane : 4 * mem, 0, 16);
      const unsigned base = __builtin_amdgcn_readlane(old, 24) + 1;
      unsigned used = 0;
#pragma unroll
      for (int i = 0; i < 24; ++i) { const unsigned d = __builtin_amdgcn_readlane(old, i) - base; used |= d < 32 ? 1u << d : 0u; }
      if (lane == 0) __builtin_amdgcn_raw_buffer_store_b32(base + __builtin_ctz(~used), rf, 4 * mem, 0, 16);
    }
    zero_acc();
    bf16x8 wa[8], wb[8], xa[3], xb[3];
#pragma unroll
    for (int d = 0; d < 8; ++d) wb[d] = bf16x8{};
#pragma unroll
    for (int q = 0; q < 3; ++q) xb[q] = bf16x8{};
    unsigned bufb = ldsb;
#pragma clang loop unroll(disable)
    for (int c = 0; c < 9; ++c) {
      const unsigned a = bufb + 16384 * jj + tro, xo = bufb + xo0;
      bufb = bufb == ldsb ? ldsb + QB1 : bufb == ldsb + QB1 ? ldsb + QB2 : ldsb;
      BARRIER();
#pragma unroll
      for (int q = 0; q < 3; ++q) xa[q] = lds_b128(xo + 2048 * q);
      rd_tr8(wa, a, 0);
      mm24(wb, xb);
      weave<19>();
      SB0();
#pragma unroll
      for (int q = 0; q < 3; ++q) xb[q] = lds_b128((xo ^ 64) + 2048 * q);
      rd_tr8(wb, a, 8192);
      mm24(wa, xa);
      weave<19>();
    }
    mm24(wb, xb);
    pack_q();
  } else {
    const char* xp = blk ? xB : xA; const char* wp = blk ? wqB : wqA;
    const __amdgpu_buffer_rsrc_t rx = mkrsrc(xp + (size_t)b * SQ * DM * 2, SQ * DM * 2);
    const __amdgpu_buffer_rsrc_t rw = mkrsrc(wp, DM * HD * 2);
    unsigned vw[4], vx[3];
#pragma unroll
    for (int pp = 0; pp < 4; ++pp) vw[pp] = (4 * pp + g) * (HD * 2) - 1024 * pp + 16 * (c16 ^ ((g << 2) | (pp & 2)));
#pragma unroll
    for (int i = 0; i < 3; ++i) vx[i] = (8 * i + (lane >> 3)) * (DM * 2) - 1024 * i + 16 * ((lane & 7) ^ ((4 * (3 * u + i) + g) & 7));
    const unsigned qrot = (b + 4 * (idx >> 4)) % 9;
    auto x_issue = [&](unsigned c, unsigned buf) { dma3(rx, buf + 32768 + 3072 * u, vx[0], vx[1], vx[2], c * 128 + 24 * u * (DM * 2)); };
    auto w_issue = [&](unsigned c, unsigned ks, unsigned buf, unsigned head) {
      dma4(rw, buf + 16384 * jj + 8192 * ks + 4096 * qh, vw[0], vw[1], vw[2], vw[3], (c * 64 + 32 * ks + 16 * qh) * (HD * 2) + head * 256);
    };
    auto nxt = [&](unsigned c) { return c == 8 ? 0u : c + 1; };
    unsigned cq = qrot;
    x_issue(cq, ldsb); w_issue(cq, 0, ldsb, h - 2); w_issue(cq, 1, ldsb, h - 2); cq = nxt(cq);
    x_issue(cq, ldsb + QB1); w_issue(cq, 0, ldsb + QB1, h - 2); w_issue(cq, 1, ldsb + QB1, h - 2); cq = nxt(cq);
    { unsigned c = 2 + (lane >= 24) + (lane >= 48) + qrot; if (c >= 9) c -= 9;
      touch(rx, ldsb + SCR, (24 * u + lane % 24) * (DM * 2) + c * 128, 0);
      c = 4 + qrot; if (c >= 9) c -= 9;
      touch(rx, ldsb + SCR, (24 * u + 16 + (lane & 7)) * (DM * 2) + c * 128, 0); }
    unsigned bufb = ldsb + QB2;
    WAITV(13);
#pragma clang loop unroll(disable)
    for (int c = 0; c < 7; ++c) {
      BARRIER();
      x_issue(cq, bufb); w_issue(cq, 0, bufb, h - 2); w_issue(cq, 1, bufb, h - 2); cq = nxt(cq);
      bufb = bufb == ldsb ? ldsb + QB1 : bufb == ldsb + QB1 ? ldsb + QB2 : ldsb;
      WAITV(11);
    }
    k_issue(0);
    BARRIER();
    v_issue(0);
    WAITV(8); BARRIER();
    k_issue(1); v_issue(1);
    zero_acc();
    WAITV(12); BARRIER();
    x_issue(cq, ldsb + QB1); w_issue(cq, 0, ldsb + QB1, h); w_issue(cq, 1, ldsb + QB1, h); cq = nxt(cq);
    WAITV(19); BARRIER();
    k_issue(2);
    unsigned T = 1;
#pragma clang loop unroll(disable)
    for (int c = 0; c < 9; ++c) {
      const unsigned qb = ldsb + ((c & 1) ? QB2 : QB1), qn = ldsb + ((c & 1) ? QB1 : QB2);
      const bool fast = c & 1;
      bf16x8 wf[2][8], xf[2][3];
      if (c == 0) WAITV(4); else WAITV(8);
      BARRIER();
#pragma unroll
      for (int q = 0; q < 3; ++q) xf[0][q] = lds_b128(qb + xo0 + 2048 * q);
      rd_tr8(wf[0], qb + 16384 * jj + tro, 0);
      if (!fast) {
        if (c < 8) { x_issue(cq, qn); w_issue(cq, 0, qn, h); }
        v_issue(T + 1);
        if (c < 8) WAITV(15); else WAITV(8);
        BARRIER();
        k_issue(T + 2); ++T;
        mm24(wf[0], xf[0]);
        if (c < 8) WAITV(15); else WAITV(8);
        BARRIER();
      } else { x_issue(cq, qn); w_issue(cq, 0, qn, h); }
#pragma unroll
      for (int q = 0; q < 3; ++q) xf[1][q] = lds_b128(((qb + xo0) ^ 64) + 2048 * q);
      rd_tr8(wf[1], qb + 16384 * jj + tro, 8192);
      if (c < 8) w_issue(cq, 1, qn, h);
      cq = nxt(cq);
      v_issue(T + 1);
      if (fast) WAITV(19); else if (c < 8) WAITV(12); else WAITV(8);
      BARRIER();
      k_issue(T + 2); ++T;
      if (fast) mm24(wf[0], xf[0]);
      mm24(wf[1], xf[1]);
    }
    WAITV(8); BARRIER();
    v_issue(16);
    pack_q();
    WAITV(8); BARRIER();
    k_issue(17);
  }

  const unsigned prot = (b >> 3) + (b & 1), ksrot = b & 7;
  unsigned vo[3];
#pragma unroll
  for (int i = 0; i < 3; ++i) { const unsigned L = 64 * i + lane, row = L / 24, ph = L - 24 * row; vo[i] = row * (DM * 2) + 16 * (ph ^ (row & 6)) - 1024 * i; }
  unsigned wo_tab;
  { const unsigned r = lane >= 24, k = lane - 24 * r; unsigned p = (k >> 3) + prot; if (p >= 3) p -= 3;
    wo_tab = ((hb + 2 * r) * DH + 32 * ((k + ksrot) & 7) + 8 * u) * (DM * 2) + 384 * p; }
  auto wo_issue = [&](unsigned i, unsigned buf) { dma3w(ro, buf + 3072 * u, vo[0], vo[1], vo[2], __builtin_amdgcn_readlane(wo_tab, i)); };

  {
    zero_acc();
    f32x4 negm[3] = {f32x4{}, f32x4{}, f32x4{}};
    float l[3] = {0.f, 0.f, 0.f};
    const unsigned koff = ldsb + KVS + 8192 * jj + 256 * (8 * q4 + p4) + 16 * (g ^ c16), voff = ldsb + 8192 * jj + tro;
    f32x4 sc[2][3];
    bf16x8 kf[2][4];
    auto qk = [&]() {
#pragma unroll
      for (int kb = 0; kb < 2; ++kb)
#pragma unroll
        for (int q = 0; q < 3; ++q) sc[kb][q] = MFMA16(kf[kb][0], qf[0][q], negm[q]);
#pragma unroll
      for (int s = 1; s < 4; ++s)
#pragma unroll
        for (int kb = 0; kb < 2; ++kb)
#pragma unroll
          for (int q = 0; q < 3; ++q) sc[kb][q] = MFMA16(kf[kb][s], qf[s][q], sc[kb][q]);
    };
    WAITV(4);
    BARRIER();
#pragma unroll
    for (int s = 0; s < 4; ++s) { const unsigned a = (2 * KVS + koff) ^ (64 * s); kf[0][s] = lds_b128(a); kf[1][s] = lds_b128(a + 1024); }
    qk();
#pragma clang loop unroll(disable)
    for (int t = 0; t < 16; ++t) {
      const unsigned sb = (t & 1) * (2 * KVS);
      BARRIER();
      bf16x8 vf[8];
      rd_tr8(vf, sb + voff, 0);
      MEMFENCE(); SB0();
      float mx[3];
#pragma unroll
      for (int q = 0; q < 3; ++q)
        mx[q] = max3f(max3f(sc[0][q][0], sc[0][q][1], sc[0][q][2]), max3f(sc[0][q][3], sc[1][q][0], sc[1][q][1]), max2f(sc[1][q][2], sc[1][q][3]));
      if (t == 0 || __builtin_expect(__any(!(max3f(mx[0], mx[1], mx[2]) <= THR)), 0)) {
#pragma unroll
        for (int q = 0; q < 3; ++q) {
          float dl = xg_max(mx[q]);
          if (t != 0) dl = max2f(dl, 0.f);
          const float al = t == 0 ? 0.f : __builtin_amdgcn_exp2f(-dl);
          negm[q] -= dl; sc[0][q] -= dl; sc[1][q] -= dl; l[q] *= al;
#pragma unroll
          for (int d = 0; d < 8; ++d) acc[d][q] *= al;
        }
      }
      bf16x8 pf[3];
#pragma unroll
      for (int q = 0; q < 3; ++q) {
#pragma unroll
        for (int i = 0; i < 4; ++i) { sc[0][q][i] = __builtin_amdgcn_exp2f(sc[0][q][i]); sc[1][q][i] = __builtin_amdgcn_exp2f(sc[1][q][i]); }
        l[q] += ((sc[0][q][0] + sc[0][q][1]) + (sc[0][q][2] + sc[0][q][3])) + ((sc[1][q][0] + sc[1][q][1]) + (sc[1][q][2] + sc[1][q][3]));
        pf[q] = pack2(sc[0][q], sc[1][q]);
      }
      asm volatile("" : "+v"(pf[0]), "+v"(pf[1]), "+v"(pf[2]), "+v"(l[0]), "+v"(l[1]), "+v"(l[2]));
      WAITV(0); BARRIER();
      if (t == 15 && team == 0) { wo_issue(0, ldsb + WREG); wo_issue(1, ldsb + WREG + WOU); v_issue(17); }
#pragma unroll
      for (int d = 0; d < 8; ++d) {
#pragma unroll
        for (int q = 0; q < 3; ++q) acc[d][q] = MFMA16(vf[d], pf[q], acc[d][q]);
        if (d >= 2 && d < 6) { const unsigned a = (sb + koff) ^ (64 * (d - 2)); kf[0][d - 2] = lds_b128(a); kf[1][d - 2] = lds_b128(a + 1024); MEMFENCE(); }
        SB0();
      }
      if (t < 15) qk();
    }
#pragma unroll
    for (int q = 0; q < 3; ++q) {
      const float inv = 1.0f / xg_sum(l[q]);
#pragma unroll
      for (int s = 0; s < 4; ++s)
        *(LAS bf16x8*)(uintptr_t)(ldsb + OREG + (((4 * jj + s - ksrot) & 7) * 6 + 3 * qh + q) * 1024 + lane * 16) = pack2(acc[2 * s][q] * inv, acc[2 * s + 1][q] * inv);
    }
  }

  const __amdgpu_buffer_rsrc_t rg = mkrsrc(ows + (size_t)grp * 8 * PART, 8 * PART);
  auto await_pass = [&](unsigned pi) {
    const unsigned mine = rfl(__builtin_amdgcn_raw_buffer_load_b32(rf, 4 * mem, 0, 16));
#pragma clang loop unroll(disable)
    for (unsigned spin = 0; spin < (1u << 22); ++spin) {
      const unsigned ec = __builtin_amdgcn_raw_buffer_load_b32(rf, 128 * (1 + mem) + 4 * (lane & 7), 32 * pi, 16);
      MEMFENCE();
      if (__all(ec == mine)) break;
      __builtin_amdgcn_s_sleep(1);
    }
  };
  auto sum_pass = [&](auto NS_, unsigned NL, unsigned pi, unsigned r1) {
    constexpr int NS = NS_.value;
    unsigned p = pi + prot; if (p >= 3) p -= 3;
    if (lane < NL) {
      const unsigned r0 = mem * 288 + r1 + lane;
      u32x4 v[NS][8];
#pragma unroll
      for (int s = 0; s < NS; ++s)
#pragma unroll
        for (int i = 0; i < 8; ++i) v[s][i] = __builtin_amdgcn_raw_buffer_load_b128(rg, (r0 + NL * s) * 16, i * PART + p * 36864, 16);
#pragma unroll
      for (int s = 0; s < NS; ++s) {
        float a[8] = {0.f, 0.f, 0.f, 0.f, 0.f, 0.f, 0.f, 0.f};
#pragma unroll
        for (int i = 0; i < 8; ++i)
#pragma unroll
          for (int k = 0; k < 4; ++k) { a[2 * k] += __uint_as_float(v[s][i][k] << 16); a[2 * k + 1] += __uint_as_float(v[s][i][k] & 0xffff0000u); }
        const unsigned r = r0 + NL * s, ln = r & 63, f = r >> 6, uu = f / 9, i9 = f - 9 * uu, np = i9 / 3, q = i9 - 3 * np;
        const unsigned row = 48 * (uu & 1) + 16 * q + (ln & 15), col = 192 * p + 96 * (uu >> 1) + 32 * np + 8 * (ln >> 4);
        *(u32x4*)(out + ((size_t)(b * SQ + row) * (2 * DM) + blk * DM + col) * 2) = u32x4{cvtpk(a[0], a[1]), cvtpk(a[2], a[3]), cvtpk(a[4], a[5]), cvtpk(a[6], a[7])};
      }
    }
  };

  if (team == 0) {
    unsigned wof[6];
#pragma unroll
    for (int n = 0; n < 6; ++n) wof[n] = ldsb + (4 * g + q4) * 384 + 16 * ((12 * jj + 2 * n + (p4 >> 1)) ^ ((4 * g + q4) & 6)) + 8 * (p4 & 1);
    const unsigned oof = ldsb + OREG + 3 * qh * 1024 + lane * 16;
    f32x4 oc[6][3];
    unsigned pA[36], pB[36], pC[36];
#pragma unroll
    for (int i = 0; i < 36; ++i) { pB[i] = 0; pC[i] = 0; }
    auto mm18 = [&](const bf16x8 (&w)[6], const bf16x8 (&o)[3]) {
#pragma unroll
      for (int n = 0; n < 6; ++n)
#pragma unroll
        for (int q = 0; q < 3; ++q) oc[n][q] = MFMA16(w[n], o[q], oc[n][q]);
    };
    BARRIER(); wo_issue(2, ldsb + WREG + 2 * WOU); k_issue(18);
    unsigned T = 17;
#pragma clang loop unroll(disable)
    for (int p = 0; p < 3; ++p) {
#pragma unroll
      for (int n = 0; n < 6; ++n)
#pragma unroll
        for (int q = 0; q < 3; ++q) oc[n][q] = f32x4{};
#pragma clang loop unroll(disable)
      for (unsigned k = 0; k < 8; k += 2, ++T) {
        WAITV(11); BARRIER();
        bf16x8 of[2][3], wf[2][6];
        const unsigned wb = WREG + (k & 2) * WOU, ob = oof + k * 6144;
#pragma unroll
        for (int i = 0; i < 2; ++i) {
#pragma unroll
          for (int q = 0; q < 3; ++q) of[i][q] = lds_b128(ob + 6144 * i + 1024 * q);
#pragma unroll
          for (int n = 0; n < 6; ++n) wf[i][n] = lds_tr2(wb + wof[n] + WOU * i, wb + wof[n] + WOU * i + 16 * 384);
        }
        wo_issue(2 * T - 31, ldsb + WREG + ((k & 2) ^ 3) * WOU);
        v_issue(T + 1);
        WAITV(14); BARRIER();
        wo_issue(2 * T - 30, ldsb + wb);
        k_issue(T + 2);
        mm18(wf[0], of[0]); mm18(wf[1], of[1]);
      }
#pragma unroll
      for (int i = 0; i < 36; ++i) {
        const f32x4 v = oc[i / 6][(i / 2) % 3];
        pA[i] = pB[i]; pB[i] = pC[i]; pC[i] = cvtpk(v[2 * (i & 1)], v[2 * (i & 1) + 1]);
      }
    }
    WAITV(14); BARRIER(); wo_issue(27, ldsb + WREG + 3 * WOU); v_issue(30);
    WAITV(14); BARRIER(); k_issue(31);
    WAITV(11); BARRIER(); v_issue(31);
    auto unpark = [&](const unsigned (&p)[36]) {
#pragma unroll
      for (int i = 0; i < 18; ++i) {
        unsigned v0 = p[2 * i], v1 = p[2 * i + 1]; asm volatile("" : "+v"(v0), "+v"(v1));
        oc[i / 3][i % 3] = f32x4{__uint_as_float(v0 << 16), __uint_as_float(v0 & 0xffff0000u), __uint_as_float(v1 << 16), __uint_as_float(v1 & 0xffff0000u)};
      }
    };
    WAITV(8); BARRIER();
    unpark(pA);
    unsigned woh[6];
#pragma unroll
    for (int n = 0; n < 6; ++n) { woh[n] = wof[n] + WREG; asm volatile("" : "+v"(woh[n])); }
    bf16x8 wa[6], wb[6], oa[3], ob[3];
    WAITV(4); BARRIER();
    WAITV(0); BARRIER();
    wo_issue(28, ldsb); wo_issue(29, ldsb + WOU);
    BARRIER();
    wo_issue(30, ldsb + 2 * WOU); wo_issue(31, ldsb + 3 * WOU);
    const __amdgpu_buffer_rsrc_t rp = mkrsrc(ows + (size_t)(grp * 8 + mem) * PART, PART);
    unsigned tick = __builtin_amdgcn_raw_buffer_load_b32(rf, 4 * (2 * u + (lane & 1)), 0, 16);
    auto publish = [&](unsigned pi) { __builtin_amdgcn_raw_buffer_store_b32(tick, rf, lane < 2 ? 128 * (1 + 2 * u + lane) + 4 * mem : FLG, 32 * pi, 16); };
    auto store_pass = [&](unsigned pi) {
      unsigned p = pi + prot; if (p >= 3) p -= 3;
#pragma unroll
      for (int n = 0; n < 3; ++n)
#pragma unroll
        for (int q = 0; q < 3; ++q) __builtin_amdgcn_raw_buffer_store_b128(__builtin_bit_cast(u32x4, pack_swap(oc[2 * n][q], oc[2 * n + 1][q])), rp, lane * 16, p * 36864 + u * 9216 + (n * 3 + q) * 1024, 16);
    };
    auto rd_unit = [&](bf16x8 (&w)[6], bf16x8 (&o)[3], const int k) {
#pragma unroll
      for (int q = 0; q < 3; ++q) o[q] = lds_b128(oof + k * 6144 + 1024 * q);
#pragma unroll
      for (int n = 0; n < 6; ++n) { const unsigned a = (k < 4 ? woh[n] : wof[n]) + (k & 3) * WOU; w[n] = lds_tr2(a, a + 16 * 384); }
    };
    BARRIER();
    rd_unit(wa, oa, 0);
#pragma clang loop unroll(disable)
    for (unsigned p = 0; p < 3; ++p) {
#pragma unroll
      for (int j = 0; j < 4; ++j) {
        rd_unit(wb, ob, 2 * j + 1);
        mm18(wa, oa);
        weave<15>();
        MEMFENCE(); SB0();
        if (j == 1) { WAITV(0); publish(p > 0 ? p - 1 : 64); }
        if (j < 2) asm volatile("s_barrier" ::: "memory");
        else asm volatile("s_cmp_eq_u32 %0, 2\n\ts_cbranch_scc1 1f\n\ts_barrier\n1:" :: "s"(p) : "scc", "memory");
        SB0();
        rd_unit(wa, oa, (2 * j + 2) & 7);
        mm18(wb, ob);
        weave<15>();
        MEMFENCE(); SB0();
      }
      store_pass(p);
      if (p == 0) unpark(pB); else unpark(pC);
    }
    WAITV(0);
    publish(2);
    await_pass(2);
    const unsigned now = __builtin_amdgcn_raw_buffer_load_b32(rf, 4 * (2 * u + (lane & 1)), 0, 16);
    sum_pass(std::integral_constant<int, 2>{}, 36, 2, 72 * u);
    if (now != tick) { tick = now; publish(0); publish(1); publish(2); }
  } else {
#pragma clang loop unroll(disable)
    for (unsigned j = 0; j < 11; ++j) {
      if (j < 10) WAITV(9); else WAITV(0);
      BARRIER();
      const unsigned k = 2 * j + 5;
      if (j >= 2 && j < 10) wo_issue(24 + k, ldsb + ((k & 4) ? 0 : WREG) + (k & 3) * WOU);
      if (j >= 1 && j < 9) wo_issue(25 + k, ldsb + (((k + 1) & 4) ? 0 : WREG) + ((k + 1) & 3) * WOU);
    }
    await_pass(jj);
    sum_pass(std::integral_constant<int, 3>{}, 48, jj, 144 * qh);
  }
  asm volatile("s_waitcnt vmcnt(0)\nattn_code_end:" ::: "memory");
}

extern "C" void kernel_launch(void* const* d_in, const int* in_sizes, int n_in, void* d_out, int out_size, void* d_ws, size_t ws_size,
                              hipStream_t stream) {
  (void)in_sizes; (void)n_in; (void)out_size; (void)ws_size;
  (void)hipFuncSetAttribute((const void*)attn_main, hipFuncAttributeMaxDynamicSharedMemorySize, LDS_MAIN);
  attn_main<<<256, 512, LDS_MAIN, stream>>>((const char*)d_in[0], (const char*)d_in[1], (const char*)d_in[2], (const char*)d_in[3], (const char*)d_in[4],
                                            (const char*)d_in[5], (const char*)d_in[6], (const char*)d_in[7], (const char*)d_in[8], (const char*)d_in[9], (char*)d_ws, (char*)d_out);
}
```

```cpp
#include <hip/hip_runtime.h>
#include <hip/hip_bf16.h>
#include <stdint.h>
#include <type_traits>

#define LAS __attribute__((address_space(3)))
#define DI __device__ __forceinline__
typedef __bf16 bf16x2 __attribute__((ext_vector_type(2)));
typedef __bf16 bf16x4 __attribute__((ext_vector_type(4)));
typedef __bf16 bf16x8 __attribute__((ext_vector_type(8)));
typedef float f32x2 __attribute__((ext_vector_type(2)));
typedef float f32x4 __attribute__((ext_vector_type(4)));
typedef unsigned u32x4 __attribute__((ext_vector_type(4)));
typedef unsigned u32x2 __attribute__((ext_vector_type(2)));

constexpr int NB = 16, SQ = 96, DM = 576, NH = 32, SK = 512, DH = 128, HD = NH * DH;
constexpr unsigned LDS_MAIN = 163840;
constexpr float SC_LOG2E = 0.08838834764831845f * 1.4426950408889634f;
constexpr float THR = 8.0f;

DI unsigned rfl(unsigned x) { return __builtin_amdgcn_readfirstlane(x); }
DI unsigned cvtpk(float lo, float hi) { f32x2 v = {lo, hi}; bf16x2 b = __builtin_convertvector(v, bf16x2); return __builtin_bit_cast(unsigned, b); }
DI bf16x8 pack2(const f32x4& a, const f32x4& b) {
  u32x4 w = {cvtpk(a[0], a[1]), cvtpk(a[2], a[3]), cvtpk(b[0], b[1]), cvtpk(b[2], b[3])};
  return __builtin_bit_cast(bf16x8, w);
}
DI bf16x8 pack_swap(const f32x4& a0, const f32x4& a1) {
  unsigned p00 = cvtpk(a0[0], a0[1]), p01 = cvtpk(a0[2], a0[3]), p10 = cvtpk(a1[0], a1[1]), p11 = cvtpk(a1[2], a1[3]);
  auto r0 = __builtin_amdgcn_permlane32_swap(p00, p10, false, false);
  auto r1 = __builtin_amdgcn_permlane32_swap(p01, p11, false, false);
  auto t0 = __builtin_amdgcn_permlane16_swap(r0[0], r0[1], false, false);
  auto t1 = __builtin_amdgcn_permlane16_swap(r1[0], r1[1], false, false);
  u32x4 w = {t0[0], t1[0], t0[1], t1[1]};
  return __builtin_bit_cast(bf16x8, w);
}
DI bf16x8 lds_b128(unsigned a) { return *(const LAS bf16x8*)(uintptr_t)a; }
DI bf16x8 lds_tr2(unsigned a0, unsigned a1) {
  bf16x4 lo = __builtin_amdgcn_ds_read_tr16_b64_v4bf16((LAS bf16x4*)(uintptr_t)a0);
  bf16x4 hi = __builtin_amdgcn_ds_read_tr16_b64_v4bf16((LAS bf16x4*)(uintptr_t)a1);
  return __builtin_shufflevector(lo, hi, 0, 1, 2, 3, 4, 5, 6, 7);
}
DI float max2f(float a, float b) { return __builtin_elementwise_maximum(a, b); }
DI float max3f(float a, float b, float c) { return max2f(max2f(a, b), c); }
DI float xg_max(float v) {
  auto r = __builtin_amdgcn_permlane32_swap(__float_as_uint(v), __float_as_uint(v), false, false); v = max2f(__uint_as_float(r[0]), __uint_as_float(r[1]));
  auto s = __builtin_amdgcn_permlane16_swap(__float_as_uint(v), __float_as_uint(v), false, false); return max2f(__uint_as_float(s[0]), __uint_as_float(s[1]));
}
DI float xg_sum(float v) {
  auto r = __builtin_amdgcn_permlane32_swap(__float_as_uint(v), __float_as_uint(v), false, false); v = __uint_as_float(r[0]) + __uint_as_float(r[1]);
  auto s = __builtin_amdgcn_permlane16_swap(__float_as_uint(v), __float_as_uint(v), false, false); return __uint_as_float(s[0]) + __uint_as_float(s[1]);
}
#define MFMA16(a, b, c) __builtin_amdgcn_mfma_f32_16x16x32_bf16((a), (b), (c), 0, 0, 0)
#define SB0() __builtin_amdgcn_sched_barrier(0)
#define MEMFENCE() asm volatile("" ::: "memory")
#define BARRIER() do { SB0(); __builtin_amdgcn_s_waitcnt(0xC07F); asm volatile("s_barrier" ::: "memory"); SB0(); } while (0)
#define WAITV(n) asm volatile("s_waitcnt vmcnt(" #n ")" ::: "memory")
template <int N> DI void weave() {
#pragma unroll
  for (int i = 0; i < N; ++i) { __builtin_amdgcn_sched_group_barrier(0x8, 1, 0); __builtin_amdgcn_sched_group_barrier(0x100, 1, 0); }
}
DI __amdgpu_buffer_rsrc_t mkrsrc(const void* p, unsigned bytes) { return __builtin_amdgcn_make_buffer_rsrc((void*)p, (short)0, (int)bytes, 0x00020000); }
DI void dma4_nt(__amdgpu_buffer_rsrc_t r, unsigned lds, unsigned va, unsigned vb, unsigned so) {
  asm volatile("s_mov_b32 m0, %0\n\ts_nop 0\n\t"
               "buffer_load_dwordx4 %1, %3, %4 offen lds\n\t"
               "buffer_load_dwordx4 %1, %3, %4 offen offset:1024 lds\n\t"
               "buffer_load_dwordx4 %2, %3, %4 offen offset:2048 lds\n\t"
               "buffer_load_dwordx4 %2, %3, %4 offen offset:3072 lds"
               :: "s"(lds), "v"(va), "v"(vb), "s"(r), "s"(so) : "memory");
}
DI void dma4(__amdgpu_buffer_rsrc_t r, unsigned lds, unsigned v0, unsigned v1, unsigned v2, unsigned v3, unsigned so) {
  asm volatile("s_mov_b32 m0, %0\n\ts_nop 0\n\t"
               "buffer_load_dwordx4 %1, %5, %6 offen lds\n\t"
               "buffer_load_dwordx4 %2, %5, %6 offen offset:1024 lds\n\t"
               "buffer_load_dwordx4 %3, %5, %6 offen offset:2048 lds\n\t"
               "buffer_load_dwordx4 %4, %5, %6 offen offset:3072 lds"
               :: "s"(lds), "v"(v0), "v"(v1), "v"(v2), "v"(v3), "s"(r), "s"(so) : "memory");
}
DI void dma3(__amdgpu_buffer_rsrc_t r, unsigned lds, unsigned v0, unsigned v1, unsigned v2, unsigned so) {
  asm volatile("s_mov_b32 m0, %0\n\ts_nop 0\n\t"
               "buffer_load_dwordx4 %1, %4, %5 offen lds\n\t"
               "buffer_load_dwordx4 %2, %4, %5 offen offset:1024 lds\n\t"
               "buffer_load_dwordx4 %3, %4, %5 offen offset:2048 lds"
               :: "s"(lds), "v"(v0), "v"(v1), "v"(v2), "s"(r), "s"(so) : "memory");
}
DI void dma3w(__amdgpu_buffer_rsrc_t r, unsigned lds, unsigned v0, unsigned v1, unsigned v2, unsigned so) {
  asm volatile("s_mov_b32 m0, %0\n\ts_nop 3\n\t"
               "buffer_load_dwordx4 %1, %4, %5 offen lds\n\t"
               "buffer_load_dwordx4 %2, %4, %5 offen offset:1024 lds\n\t"
               "buffer_load_dwordx4 %3, %4, %5 offen offset:2048 lds"
               :: "s"(lds), "v"(v0), "v"(v1), "v"(v2), "s"(r), "s"(so) : "memory");
}
DI void touch(__amdgpu_buffer_rsrc_t r, unsigned lds, unsigned v, unsigned so) {
  asm volatile("s_mov_b32 m0, %0\n\ts_nop 0\n\tbuffer_load_dword %1, %2, %3 offen lds" :: "s"(lds), "v"(v), "s"(r), "s"(so) : "memory");
}

#define CODEPF 1
#define TLBPF 1
constexpr unsigned KVS = 16384, NKV = 4;
constexpr unsigned QB1 = 73728, QB2 = 118784;
constexpr unsigned SCR = 65536;
constexpr unsigned OREG = 65536, WREG = 114688, WOU = 12288;
constexpr unsigned FLG = 2048;
constexpr unsigned PART = 110592;

__global__ __launch_bounds__(512, 2) void attn_main(const char* xA, const char* kA, const char* vA, const char* wqA, const char* woA,
                                                    const char* xB, const char* kB, const char* vB, const char* wqB, const char* woB, char* ows, char* out) {
  extern __shared__ __attribute__((aligned(16))) char lds[];
  const unsigned ldsb = rfl((unsigned)(uintptr_t)(LAS char*)lds);
  const unsigned wid = rfl(threadIdx.x >> 6), lane = threadIdx.x & 63, team = wid >> 2, u = wid & 3, jj = u >> 1, qh = u & 1;
  const unsigned id = blockIdx.x, xcd = id & 7, idx = id >> 3, blk = xcd >> 2;
  const unsigned b = idx & 15, hb = 8 * (xcd & 3) + 4 * (idx >> 4), h = hb + 2 * team + jj;
  const char* kp = blk ? kB : kA; const char* vp = blk ? vB : vA; const char* wop = blk ? woB : woA;
  const __amdgpu_buffer_rsrc_t rk = mkrsrc(kp + (size_t)(b * NH + hb) * SK * DH * 2, 4 * SK * DH * 2);
  const __amdgpu_buffer_rsrc_t rv = mkrsrc(vp + (size_t)(b * NH + hb) * SK * DH * 2, 4 * SK * DH * 2);
  const __amdgpu_buffer_rsrc_t ro = mkrsrc(wop, HD * DM * 2);
  const unsigned grp = blk * NB + b, mem = hb >> 2;
  const __amdgpu_buffer_rsrc_t rf = mkrsrc(ows + (size_t)256 * PART + grp * FLG, FLG);
  const unsigned g = lane >> 4, c16 = lane & 15, q4 = c16 >> 2, p4 = lane & 3;
  const unsigned vka = g * 256 + 16 * (c16 ^ ((8 * qh) | g)), vkb = g * 256 + 16 * (c16 ^ ((8 * qh + 4) | g));
  const unsigned vva = g * 256 + 16 * (c16 ^ (g << 2)), vvb = g * 256 + 16 * (c16 ^ ((g << 2) | 2));
  const unsigned krot = (id * 5 + (id >> 3) + jj * 7) & 15;
  const unsigned kvs0 = jj * (SK * DH * 2) + 4096 * qh, kvd0 = ldsb + 8192 * jj + 4096 * qh;
  auto kv_so = [&](unsigned T) { return kvs0 + ((T & 16) << 14) + (((T + krot) & 15) << 13); };
  auto k_issue = [&](unsigned T) { dma4_nt(rk, kvd0 + 3 * KVS - (T & 1) * (2 * KVS), vka, vkb, kv_so(T)); };
  auto v_issue = [&](unsigned T) { dma4_nt(rv, kvd0 + (T & 1) * (2 * KVS), vva, vvb, kv_so(T)); };
  const unsigned tro = 256 * (8 * g + q4) + 16 * ((q4 << 2) | ((g & 1) << 1) | (p4 >> 1)) + 8 * (p4 & 1);
  const unsigned xo0 = 32768 + (48 * qh + c16) * 128 + 16 * (g ^ ((c16 >> 1) & 7));
  auto rd_tr8 = [&](bf16x8 (&f)[8], unsigned a, unsigned imm) {
#pragma unroll
    for (int d = 0; d < 8; ++d) f[d] = lds_tr2((a ^ (32 * d)) + imm, (a ^ (32 * d)) + imm + 1024);
  };

  bf16x8 qf[4][3];
  f32x4 acc[8][3];
  auto zero_acc = [&]() {
#pragma unroll
    for (int d = 0; d < 8; ++d)
#pragma unroll
      for (int q = 0; q < 3; ++q) acc[d][q] = f32x4{};
  };
  auto mm24 = [&](const bf16x8 (&a)[8], const bf16x8 (&x)[3]) {
#pragma unroll
    for (int d = 0; d < 8; ++d)
#pragma unroll
      for (int q = 0; q < 3; ++q) acc[d][q] = MFMA16(a[d], x[q], acc[d][q]);
  };
  auto pack_q = [&]() {
#pragma unroll
    for (int s = 0; s < 4; ++s)
#pragma unroll
      for (int q = 0; q < 3; ++q) qf[s][q] = pack_swap(acc[2 * s][q] * SC_LOG2E, acc[2 * s + 1][q] * SC_LOG2E);
  };

  unsigned long long pc0; unsigned clen;
  asm volatile("attn_code_begin:\n\ts_getpc_b64 %0\n\ts_mov_b32 %1, attn_code_end-attn_code_begin-8" : "=s"(pc0), "=s"(clen));
  if (team == 0) {
    if (u == 1) touch(mkrsrc((const void*)pc0, clen), ldsb + SCR + 256, (idx * 8 + (lane & 7)) * 128, 0);
    if (u == 2) { touch(rk, ldsb + SCR + 512, (lane & 3) * 131072, 0); touch(rv, ldsb + SCR + 512, (lane & 3) * 131072, 0); }
    if (u == 3) { touch(ro, ldsb + SCR + 512, 0, hb * DH * (DM * 2)); touch(mkrsrc(ows + (size_t)grp * 8 * PART, 8 * PART), ldsb + SCR + 512, (lane & 7) * PART, 0); touch(mkrsrc(out + (size_t)b * SQ * 2 * DM * 2, 4096), ldsb + SCR + 512, 0, 0); }
    if (u == 0) {
      const unsigned old = __builtin_amdgcn_raw_buffer_load_b32(rf, lane < 24 ? 128 * (1 + mem) + 4 * lane : 4 * mem, 0, 16);
      const unsigned base = __builtin_amdgcn_readlane(old, 24) + 1;
      unsigned used = 0;
#pragma unroll
      for (int i = 0; i < 24; ++i) { const unsigned d = __builtin_amdgcn_readlane(old, i) - base; used |= d < 32 ? 1u << d : 0u; }
      if (lane == 0) __builtin_amdgcn_raw_buffer_store_b32(base + __builtin_ctz(~used), rf, 4 * mem, 0, 16);
    }
    zero_acc();
    bf16x8 wa[8], wb[8], xa[3], xb[3];
#pragma unroll
    for (int d = 0; d < 8; ++d) wb[d] = bf16x8{};
#pragma unroll
    for (int q = 0; q < 3; ++q) xb[q] = bf16x8{};
    unsigned bufb = ldsb;
#pragma clang loop unroll(disable)
    for (int c = 0; c < 9; ++c) {
      const unsigned a = bufb + 16384 * jj + tro, xo = bufb + xo0;
      bufb = bufb == ldsb ? ldsb + QB1 : bufb == ldsb + QB1 ? ldsb + QB2 : ldsb;
      BARRIER();
#pragma unroll
      for (int q = 0; q < 3; ++q) xa[q] = lds_b128(xo + 2048 * q);
      rd_tr8(wa, a, 0);
      mm24(wb, xb);
      weave<19>();
      SB0();
#pragma unroll
      for (int q = 0; q < 3; ++q) xb[q] = lds_b128((xo ^ 64) + 2048 * q);
      rd_tr8(wb, a, 8192);
      mm24(wa, xa);
      weave<19>();
    }
    mm24(wb, xb);
    pack_q();
  } else {
    const char* xp = blk ? xB : xA; const char* wp = blk ? wqB : wqA;
    const __amdgpu_buffer_rsrc_t rx = mkrsrc(xp + (size_t)b * SQ * DM * 2, SQ * DM * 2);
    const __amdgpu_buffer_rsrc_t rw = mkrsrc(wp, DM * HD * 2);
    unsigned vw[4], vx[3];
#pragma unroll
    for (int pp = 0; pp < 4; ++pp) vw[pp] = (4 * pp + g) * (HD * 2) - 1024 * pp + 16 * (c16 ^ ((g << 2) | (pp & 2)));
#pragma unroll
    for (int i = 0; i < 3; ++i) vx[i] = (8 * i + (lane >> 3)) * (DM * 2) - 1024 * i + 16 * ((lane & 7) ^ ((4 * (3 * u + i) + g) & 7));
    const unsigned qrot = (b + 4 * (idx >> 4)) % 9;
    auto x_issue = [&](unsigned c, unsigned buf) { dma3(rx, buf + 32768 + 3072 * u, vx[0], vx[1], vx[2], c * 128 + 24 * u * (DM * 2)); };
    auto w_issue = [&](unsigned c, unsigned ks, unsigned buf, unsigned head) {
      dma4(rw, buf + 16384 * jj + 8192 * ks + 4096 * qh, vw[0], vw[1], vw[2], vw[3], (c * 64 + 32 * ks + 16 * qh) * (HD * 2) + head * 256);
    };
    auto nxt = [&](unsigned c) { return c == 8 ? 0u : c + 1; };
    unsigned cq = qrot;
    x_issue(cq, ldsb); w_issue(cq, 0, ldsb, h - 2); w_issue(cq, 1, ldsb, h - 2); cq = nxt(cq);
    x_issue(cq, ldsb + QB1); w_issue(cq, 0, ldsb + QB1, h - 2); w_issue(cq, 1, ldsb + QB1, h - 2); cq = nxt(cq);
    { unsigned c = 2 + (lane >= 24) + (lane >= 48) + qrot; if (c >= 9) c -= 9;
      touch(rx, ldsb + SCR, (24 * u + lane % 24) * (DM * 2) + c * 128, 0);
      c = 4 + qrot; if (c >= 9) c -= 9;
      touch(rx, ldsb + SCR, (24 * u + 16 + (lane & 7)) * (DM * 2) + c * 128, 0); }
    unsigned bufb = ldsb + QB2;
    WAITV(13);
#pragma clang loop unroll(disable)
    for (int c = 0; c < 7; ++c) {
      BARRIER();
      x_issue(cq, bufb); w_issue(cq, 0, bufb, h - 2); w_issue(cq, 1, bufb, h - 2); cq = nxt(cq);
      bufb = bufb == ldsb ? ldsb + QB1 : bufb == ldsb + QB1 ? ldsb + QB2 : ldsb;
      WAITV(11);
    }
    k_issue(0);
    BARRIER();
    v_issue(0);
    WAITV(8); BARRIER();
    k_issue(1); v_issue(1);
    zero_acc();
    WAITV(12); BARRIER();
    x_issue(cq, ldsb + QB1); w_issue(cq, 0, ldsb + QB1, h); w_issue(cq, 1, ldsb + QB1, h); cq = nxt(cq);
    WAITV(19); BARRIER();
    k_issue(2);
    unsigned T = 1;
#pragma clang loop unroll(disable)
    for (int c = 0; c < 9; ++c) {
      const unsigned qb = ldsb + ((c & 1) ? QB2 : QB1), qn = ldsb + ((c & 1) ? QB1 : QB2);
      const bool fast = c & 1;
      bf16x8 wf[2][8], xf[2][3];
      if (c == 0) WAITV(4); else WAITV(8);
      BARRIER();
#pragma unroll
      for (int q = 0; q < 3; ++q) xf[0][q] = lds_b128(qb + xo0 + 2048 * q);
      rd_tr8(wf[0], qb + 16384 * jj + tro, 0);
      if (!fast) {
        if (c < 8) { x_issue(cq, qn); w_issue(cq, 0, qn, h); }
        v_issue(T + 1);
        if (c < 8) WAITV(15); else WAITV(8);
        BARRIER();
        k_issue(T + 2); ++T;
        mm24(wf[0], xf[0]);
        if (c < 8) WAITV(15); else WAITV(8);
        BARRIER();
      } else { x_issue(cq, qn); w_issue(cq, 0, qn, h); }
#pragma unroll
      for (int q = 0; q < 3; ++q) xf[1][q] = lds_b128(((qb + xo0) ^ 64) + 2048 * q);
      rd_tr8(wf[1], qb + 16384 * jj + tro, 8192);
      if (c < 8) w_issue(cq, 1, qn, h);
      cq = nxt(cq);
      v_issue(T + 1);
      if (fast) WAITV(19); else if (c < 8) WAITV(12); else WAITV(8);
      BARRIER();
      k_issue(T + 2); ++T;
      if (fast) mm24(wf[0], xf[0]);
      mm24(wf[1], xf[1]);
    }
    WAITV(8); BARRIER();
    v_issue(16);
    pack_q();
    WAITV(8); BARRIER();
    k_issue(17);
  }

  const unsigned prot = (b >> 3) + (b & 1), ksrot = b & 7;
  unsigned vo[3];
#pragma unroll
  for (int i = 0; i < 3; ++i) { const unsigned L = 64 * i + lane, row = L / 24, ph = L - 24 * row; vo[i] = row * (DM * 2) + 16 * (ph ^ (row & 6)) - 1024 * i; }
  unsigned wo_tab;
  { const unsigned r = lane >= 24, k = lane - 24 * r; unsigned p = (k >> 3) + prot; if (p >= 3) p -= 3;
    wo_tab = ((hb + 2 * r) * DH + 32 * ((k + ksrot) & 7) + 8 * u) * (DM * 2) + 384 * p; }
  auto wo_issue = [&](unsigned i, unsigned buf) { dma3w(ro, buf + 3072 * u, vo[0], vo[1], vo[2], __builtin_amdgcn_readlane(wo_tab, i)); };

  {
    zero_acc();
    f32x4 negm[3] = {f32x4{}, f32x4{}, f32x4{}};
    float l[3] = {0.f, 0.f, 0.f};
    const unsigned koff = ldsb + KVS + 8192 * jj + 256 * (8 * q4 + p4) + 16 * (g ^ c16), voff = ldsb + 8192 * jj + tro;
    f32x4 sc[2][3];
    bf16x8 kf[2][4];
    auto qk = [&]() {
#pragma unroll
      for (int kb = 0; kb < 2; ++kb)
#pragma unroll
        for (int q = 0; q < 3; ++q) sc[kb][q] = MFMA16(kf[kb][0], qf[0][q], negm[q]);
#pragma unroll
      for (int s = 1; s < 4; ++s)
#pragma unroll
        for (int kb = 0; kb < 2; ++kb)
#pragma unroll
          for (int q = 0; q < 3; ++q) sc[kb][q] = MFMA16(kf[kb][s], qf[s][q], sc[kb][q]);
    };
    WAITV(4);
    BARRIER();
#pragma unroll
    for (int s = 0; s < 4; ++s) { const unsigned a = (2 * KVS + koff) ^ (64 * s); kf[0][s] = lds_b128(a); kf[1][s] = lds_b128(a + 1024); }
    qk();
#pragma clang loop unroll(disable)
    for (int t = 0; t < 16; ++t) {
      const unsigned sb = (t & 1) * (2 * KVS);
      BARRIER();
      bf16x8 vf[8];
      rd_tr8(vf, sb + voff, 0);
      MEMFENCE(); SB0();
      float mx[3];
#pragma unroll
      for (int q = 0; q < 3; ++q)
        mx[q] = max3f(max3f(sc[0][q][0], sc[0][q][1], sc[0][q][2]), max3f(sc[0][q][3], sc[1][q][0], sc[1][q][1]), max2f(sc[1][q][2], sc[1][q][3]));
      if (t == 0 || __builtin_expect(__any(!(max3f(mx[0], mx[1], mx[2]) <= THR)), 0)) {
#pragma unroll
        for (int q = 0; q < 3; ++q) {
          float dl = xg_max(mx[q]);
          if (t != 0) dl = max2f(dl, 0.f);
          const float al = t == 0 ? 0.f : __builtin_amdgcn_exp2f(-dl);
          negm[q] -= dl; sc[0][q] -= dl; sc[1][q] -= dl; l[q] *= al;
#pragma unroll
          for (int d = 0; d < 8; ++d) acc[d][q] *= al;
        }
      }
      bf16x8 pf[3];
#pragma unroll
      for (int q = 0; q < 3; ++q) {
#pragma unroll
        for (int i = 0; i < 4; ++i) { sc[0][q][i] = __builtin_amdgcn_exp2f(sc[0][q][i]); sc[1][q][i] = __builtin_amdgcn_exp2f(sc[1][q][i]); }
        l[q] += ((sc[0][q][0] + sc[0][q][1]) + (sc[0][q][2] + sc[0][q][3])) + ((sc[1][q][0] + sc[1][q][1]) + (sc[1][q][2] + sc[1][q][3]));
        pf[q] = pack2(sc[0][q], sc[1][q]);
      }
      asm volatile("" : "+v"(pf[0]), "+v"(pf[1]), "+v"(pf[2]), "+v"(l[0]), "+v"(l[1]), "+v"(l[2]));
      WAITV(0); BARRIER();
      if (t == 15 && team == 0) { wo_issue(0, ldsb + WREG); wo_issue(1, ldsb + WREG + WOU); v_issue(17); }
#pragma unroll
      for (int d = 0; d < 8; ++d) {
#pragma unroll
        for (int q = 0; q < 3; ++q) acc[d][q] = MFMA16(vf[d], pf[q], acc[d][q]);
        if (d >= 2 && d < 6) { const unsigned a = (sb + koff) ^ (64 * (d - 2)); kf[0][d - 2] = lds_b128(a); kf[1][d - 2] = lds_b128(a + 1024); MEMFENCE(); }
        SB0();
      }
      if (t < 15) qk();
    }
#pragma unroll
    for (int q = 0; q < 3; ++q) {
      const float inv = 1.0f / xg_sum(l[q]);
#pragma unroll
      for (int s = 0; s < 4; ++s)
        *(LAS bf16x8*)(uintptr_t)(ldsb + OREG + (((4 * jj + s - ksrot) & 7) * 6 + 3 * qh + q) * 1024 + lane * 16) = pack2(acc[2 * s][q] * inv, acc[2 * s + 1][q] * inv);
    }
  }

  const __amdgpu_buffer_rsrc_t rg = mkrsrc(ows + (size_t)grp * 8 * PART, 8 * PART);
  auto await_pass = [&](unsigned pi) {
    const unsigned mine = rfl(__builtin_amdgcn_raw_buffer_load_b32(rf, 4 * mem, 0, 16));
#pragma clang loop unroll(disable)
    for (unsigned spin = 0; spin < (1u << 22); ++spin) {
      const unsigned ec = __builtin_amdgcn_raw_buffer_load_b32(rf, 128 * (1 + mem) + 4 * (lane & 7), 32 * pi, 16);
      MEMFENCE();
      if (__all(ec == mine)) break;
      __builtin_amdgcn_s_sleep(1);
    }
  };
  auto sum_pass = [&](auto NS_, unsigned NL, unsigned pi, unsigned r1) {
    constexpr int NS = NS_.value;
    unsigned p = pi + prot; if (p >= 3) p -= 3;
    if (lane < NL) {
      const unsigned r0 = mem * 288 + r1 + lane;
      u32x4 v[NS][8];
#pragma unroll
      for (int s = 0; s < NS; ++s)
#pragma unroll
        for (int i = 0; i < 8; ++i) v[s][i] = __builtin_amdgcn_raw_buffer_load_b128(rg, (r0 + NL * s) * 16, i * PART + p * 36864, 16);
#pragma unroll
      for (int s = 0; s < NS; ++s) {
        float a[8] = {0.f, 0.f, 0.f, 0.f, 0.f, 0.f, 0.f, 0.f};
#pragma unroll
        for (int i = 0; i < 8; ++i)
#pragma unroll
          for (int k = 0; k < 4; ++k) { a[2 * k] += __uint_as_float(v[s][i][k] << 16); a[2 * k + 1] += __uint_as_float(v[s][i][k] & 0xffff0000u); }
        const unsigned r = r0 + NL * s, ln = r & 63, f = r >> 6, uu = f / 9, i9 = f - 9 * uu, np = i9 / 3, q = i9 - 3 * np;
        const unsigned row = 48 * (uu & 1) + 16 * q + (ln & 15), col = 192 * p + 96 * (uu >> 1) + 32 * np + 8 * (ln >> 4);
        *(u32x4*)(out + ((size_t)(b * SQ + row) * (2 * DM) + blk * DM + col) * 2) = u32x4{cvtpk(a[0], a[1]), cvtpk(a[2], a[3]), cvtpk(a[4], a[5]), cvtpk(a[6], a[7])};
      }
    }
  };

  if (team == 0) {
    unsigned wof[6];
#pragma unroll
    for (int n = 0; n < 6; ++n) wof[n] = ldsb + (4 * g + q4) * 384 + 16 * ((12 * jj + 2 * n + (p4 >> 1)) ^ ((4 * g + q4) & 6)) + 8 * (p4 & 1);
    const unsigned oof = ldsb + OREG + 3 * qh * 1024 + lane * 16;
    f32x4 oc[6][3];
    unsigned pA[36], pB[36], pC[36];
#pragma unroll
    for (int i = 0; i < 36; ++i) { pB[i] = 0; pC[i] = 0; }
    auto mm18 = [&](const bf16x8 (&w)[6], const bf16x8 (&o)[3]) {
#pragma unroll
      for (int n = 0; n < 6; ++n)
#pragma unroll
        for (int q = 0; q < 3; ++q) oc[n][q] = MFMA16(w[n], o[q], oc[n][q]);
    };
    BARRIER(); wo_issue(2, ldsb + WREG + 2 * WOU); k_issue(18);
    unsigned T = 17;
#pragma clang loop unroll(disable)
    for (int p = 0; p < 3; ++p) {
#pragma unroll
      for (int n = 0; n < 6; ++n)
#pragma unroll
        for (int q = 0; q < 3; ++q) oc[n][q] = f32x4{};
#pragma clang loop unroll(disable)
      for (unsigned k = 0; k < 8; k += 2, ++T) {
        WAITV(11); BARRIER();
        bf16x8 of[2][3], wf[2][6];
        const unsigned wb = WREG + (k & 2) * WOU, ob = oof + k * 6144;
#pragma unroll
        for (int i = 0; i < 2; ++i) {
#pragma unroll
          for (int q = 0; q < 3; ++q) of[i][q] = lds_b128(ob + 6144 * i + 1024 * q);
#pragma unroll
          for (int n = 0; n < 6; ++n) wf[i][n] = lds_tr2(wb + wof[n] + WOU * i, wb + wof[n] + WOU * i + 16 * 384);
        }
        wo_issue(2 * T - 31, ldsb + WREG + ((k & 2) ^ 3) * WOU);
        v_issue(T + 1);
        WAITV(14); BARRIER();
        wo_issue(2 * T - 30, ldsb + wb);
        k_issue(T + 2);
        mm18(wf[0], of[0]); mm18(wf[1], of[1]);
      }
#pragma unroll
      for (int i = 0; i < 36; ++i) {
        const f32x4 v = oc[i / 6][(i / 2) % 3];
        pA[i] = pB[i]; pB[i] = pC[i]; pC[i] = cvtpk(v[2 * (i & 1)], v[2 * (i & 1) + 1]);
      }
    }
    WAITV(14); BARRIER(); wo_issue(27, ldsb + WREG + 3 * WOU); v_issue(30);
    WAITV(14); BARRIER(); k_issue(31);
    WAITV(11); BARRIER(); v_issue(31);
    auto unpark = [&](const unsigned (&p)[36]) {
#pragma unroll
      for (int i = 0; i < 18; ++i) {
        unsigned v0 = p[2 * i], v1 = p[2 * i + 1]; asm volatile("" : "+v"(v0), "+v"(v1));
        oc[i / 3][i % 3] = f32x4{__uint_as_float(v0 << 16), __uint_as_float(v0 & 0xffff0000u), __uint_as_float(v1 << 16), __uint_as_float(v1 & 0xffff0000u)};
      }
    };
    WAITV(8); BARRIER();
    unpark(pA);
    unsigned woh[6];
#pragma unroll
    for (int n = 0; n < 6; ++n) { woh[n] = wof[n] + WREG; asm volatile("" : "+v"(woh[n])); }
    bf16x8 wa[6], wb[6], oa[3], ob[3];
    WAITV(4); BARRIER();
    WAITV(0); BARRIER();
    wo_issue(28, ldsb); wo_issue(29, ldsb + WOU);
    BARRIER();
    wo_issue(30, ldsb + 2 * WOU); wo_issue(31, ldsb + 3 * WOU);
    const __amdgpu_buffer_rsrc_t rp = mkrsrc(ows + (size_t)(grp * 8 + mem) * PART, PART);
    unsigned tick = __builtin_amdgcn_raw_buffer_load_b32(rf, 4 * (2 * u + (lane & 1)), 0, 16);
    auto publish = [&](unsigned pi) { __builtin_amdgcn_raw_buffer_store_b32(tick, rf, lane < 2 ? 128 * (1 + 2 * u + lane) + 4 * mem : FLG, 32 * pi, 16); };
    auto store_pass = [&](unsigned pi) {
      unsigned p = pi + prot; if (p >= 3) p -= 3;
#pragma unroll
      for (int n = 0; n < 3; ++n)
#pragma unroll
        for (int q = 0; q < 3; ++q) __builtin_amdgcn_raw_buffer_store_b128(__builtin_bit_cast(u32x4, pack_swap(oc[2 * n][q], oc[2 * n + 1][q])), rp, lane * 16, p * 36864 + u * 9216 + (n * 3 + q) * 1024, 16);
    };
    auto rd_unit = [&](bf16x8 (&w)[6], bf16x8 (&o)[3], const int k) {
#pragma unroll
      for (int q = 0; q < 3; ++q) o[q] = lds_b128(oof + k * 6144 + 1024 * q);
#pragma unroll
      for (int n = 0; n < 6; ++n) { const unsigned a = (k < 4 ? woh[n] : wof[n]) + (k & 3) * WOU; w[n] = lds_tr2(a, a + 16 * 384); }
    };
    BARRIER();
    rd_unit(wa, oa, 0);
#pragma clang loop unroll(disable)
    for (unsigned p = 0; p < 3; ++p) {
#pragma unroll
      for (int j = 0; j < 4; ++j) {
        rd_unit(wb, ob, 2 * j + 1);
        mm18(wa, oa);
        weave<15>();
        MEMFENCE(); SB0();
        if (j == 1) { WAITV(0); publish(p > 0 ? p - 1 : 64); }
        if (j < 2) asm volatile("s_barrier" ::: "memory");
        else asm volatile("s_cmp_eq_u32 %0, 2\n\ts_cbranch_scc1 1f\n\ts_barrier\n1:" :: "s"(p) : "scc", "memory");
        SB0();
        rd_unit(wa, oa, (2 * j + 2) & 7);
        mm18(wb, ob);
        weave<15>();
        MEMFENCE(); SB0();
      }
      store_pass(p);
      if (p == 0) unpark(pB); else unpark(pC);
    }
    WAITV(0);
    publish(2);
    await_pass(2);
    const unsigned now = __builtin_amdgcn_raw_buffer_load_b32(rf, 4 * (2 * u + (lane & 1)), 0, 16);
    sum_pass(std::integral_constant<int, 2>{}, 36, 2, 72 * u);
    if (now != tick) { tick = now; publish(0); publish(1); publish(2); }
  } else {
#pragma clang loop unroll(disable)
    for (unsigned j = 0; j < 11; ++j) {
      if (j < 10) WAITV(9); else WAITV(0);
      BARRIER();
      const unsigned k = 2 * j + 5;
      if (j >= 2 && j < 10) wo_issue(24 + k, ldsb + ((k & 4) ? 0 : WREG) + (k & 3) * WOU);
      if (j >= 1 && j < 9) wo_issue(25 + k, ldsb + (((k + 1) & 4) ? 0 : WREG) + ((k + 1) & 3) * WOU);
    }
    await_pass(jj);
    sum_pass(std::integral_constant<int, 3>{}, 48, jj, 144 * qh);
  }
  asm volatile("s_waitcnt vmcnt(0)\nattn_code_end:" ::: "memory");
}

extern "C" void kernel_launch(void* const* d_in, const int* in_sizes, int n_in, void* d_out, int out_size, void* d_ws, size_t ws_size,
                              hipStream_t stream) {
  (void)in_sizes; (void)n_in; (void)out_size; (void)ws_size;
  (void)hipFuncSetAttribute((const void*)attn_main, hipFuncAttributeMaxDynamicSharedMemorySize, LDS_MAIN);
  attn_main<<<256, 512, LDS_MAIN, stream>>>((const char*)d_in[0], (const char*)d_in[1], (const char*)d_in[2], (const char*)d_in[3], (const char*)d_in[4],
                                            (const char*)d_in[5], (const char*)d_in[6], (const char*)d_in[7], (const char*)d_in[8], (const char*)d_in[9], (char*)d_ws, (char*)d_out);
}
```

```cpp
#include <hip/hip_runtime.h>
#include <hip/hip_bf16.h>
#include <stdint.h>
#include <type_traits>

#define LAS __attribute__((address_space(3)))
#define DI __device__ __forceinline__
typedef __bf16 bf16x2 __attribute__((ext_vector_type(2)));
typedef __bf16 bf16x4 __attribute__((ext_vector_type(4)));
typedef __bf16 bf16x8 __attribute__((ext_vector_type(8)));
typedef float f32x2 __attribute__((ext_vector_type(2)));
typedef float f32x4 __attribute__((ext_vector_type(4)));
typedef unsigned u32x4 __attribute__((ext_vector_type(4)));
typedef unsigned u32x2 __attribute__((ext_vector_type(2)));

constexpr int NB = 16, SQ = 96, DM = 576, NH = 32, SK = 512, DH = 128, HD = NH * DH;
constexpr unsigned LDS_MAIN = 163840;
constexpr float SC_LOG2E = 0.08838834764831845f * 1.4426950408889634f;
constexpr float THR = 8.0f;

DI unsigned rfl(unsigned x) { return __builtin_amdgcn_readfirstlane(x); }
DI unsigned cvtpk(float lo, float hi) { f32x2 v = {lo, hi}; bf16x2 b = __builtin_convertvector(v, bf16x2); return __builtin_bit_cast(unsigned, b); }
DI bf16x8 pack2(const f32x4& a, const f32x4& b) {
  u32x4 w = {cvtpk(a[0], a[1]), cvtpk(a[2], a[3]), cvtpk(b[0], b[1]), cvtpk(b[2], b[3])};
  return __builtin_bit_cast(bf16x8, w);
}
DI bf16x8 pack_swap(const f32x4& a0, const f32x4& a1) {
  unsigned p00 = cvtpk(a0[0], a0[1]), p01 = cvtpk(a0[2], a0[3]), p10 = cvtpk(a1[0], a1[1]), p11 = cvtpk(a1[2], a1[3]);
  auto r0 = __builtin_amdgcn_permlane32_swap(p00, p10, false, false);
  auto r1 = __builtin_amdgcn_permlane32_swap(p01, p11, false, false);
  auto t0 = __builtin_amdgcn_permlane16_swap(r0[0], r0[1], false, false);
  auto t1 = __builtin_amdgcn_permlane16_swap(r1[0], r1[1], false, false);
  u32x4 w = {t0[0], t1[0], t0[1], t1[1]};
  return __builtin_bit_cast(bf16x8, w);
}
DI bf16x8 lds_b128(unsigned a) { return *(const LAS bf16x8*)(uintptr_t)a; }
DI bf16x8 lds_tr2(unsigned a0, unsigned a1) {
  bf16x4 lo = __builtin_amdgcn_ds_read_tr16_b64_v4bf16((LAS bf16x4*)(uintptr_t)a0);
  bf16x4 hi = __builtin_amdgcn_ds_read_tr16_b64_v4bf16((LAS bf16x4*)(uintptr_t)a1);
  return __builtin_shufflevector(lo, hi, 0, 1, 2, 3, 4, 5, 6, 7);
}
DI float max2f(float a, float b) { return __builtin_elementwise_maximum(a, b); }
DI float max3f(float a, float b, float c) { return max2f(max2f(a, b), c); }
DI float xg_max(float v) {
  auto r = __builtin_amdgcn_permlane32_swap(__float_as_uint(v), __float_as_uint(v), false, false); v = max2f(__uint_as_float(r[0]), __uint_as_float(r[1]));
  auto s = __builtin_amdgcn_permlane16_swap(__float_as_uint(v), __float_as_uint(v), false, false); return max2f(__uint_as_float(s[0]), __uint_as_float(s[1]));
}
DI float xg_sum(float v) {
  auto r = __builtin_amdgcn_permlane32_swap(__float_as_uint(v), __float_as_uint(v), false, false); v = __uint_as_float(r[0]) + __uint_as_float(r[1]);
  auto s = __builtin_amdgcn_permlane16_swap(__float_as_uint(v), __float_as_uint(v), false, false); return __uint_as_float(s[0]) + __uint_as_float(s[1]);
}
#define MFMA16(a, b, c) __builtin_amdgcn_mfma_f32_16x16x32_bf16((a), (b), (c), 0, 0, 0)
#define SB0() __builtin_amdgcn_sched_barrier(0)
#define MEMFENCE() asm volatile("" ::: "memory")
#define BARRIER() do { SB0(); __builtin_amdgcn_s_waitcnt(0xC07F); asm volatile("s_barrier" ::: "memory"); SB0(); } while (0)
#define WAITV(n) asm volatile("s_waitcnt vmcnt(" #n ")" ::: "memory")
template <int N> DI void weave() {
#pragma unroll
  for (int i = 0; i < N; ++i) { __builtin_amdgcn_sched_group_barrier(0x8, 1, 0); __builtin_amdgcn_sched_group_barrier(0x100, 1, 0); }
}
DI __amdgpu_buffer_rsrc_t mkrsrc(const void* p, unsigned bytes) { return __builtin_amdgcn_make_buffer_rsrc((void*)p, (short)0, (int)bytes, 0x00020000); }
DI void dma4_nt(__amdgpu_buffer_rsrc_t r, unsigned lds, unsigned va, unsigned vb, unsigned so) {
  asm volatile("s_mov_b32 m0, %0\n\ts_nop 0\n\t"
               "buffer_load_dwordx4 %1, %3, %4 offen nt lds\n\t"
               "buffer_load_dwordx4 %1, %3, %4 offen offset:1024 nt lds\n\t"
               "buffer_load_dwordx4 %2, %3, %4 offen offset:2048 nt lds\n\t"
               "buffer_load_dwordx4 %2, %3, %4 offen offset:3072 nt lds"
               :: "s"(lds), "v"(va), "v"(vb), "s"(r), "s"(so) : "memory");
}
DI void dma4(__amdgpu_buffer_rsrc_t r, unsigned lds, unsigned v0, unsigned v1, unsigned v2, unsigned v3, unsigned so) {
  asm volatile("s_mov_b32 m0, %0\n\ts_nop 0\n\t"
               "buffer_load_dwordx4 %1, %5, %6 offen lds\n\t"
               "buffer_load_dwordx4 %2, %5, %6 offen offset:1024 lds\n\t"
               "buffer_load_dwordx4 %3, %5, %6 offen offset:2048 lds\n\t"
               "buffer_load_dwordx4 %4, %5, %6 offen offset:3072 lds"
               :: "s"(lds), "v"(v0), "v"(v1), "v"(v2), "v"(v3), "s"(r), "s"(so) : "memory");
}
DI void dma3(__amdgpu_buffer_rsrc_t r, unsigned lds, unsigned v0, unsigned v1, unsigned v2, unsigned so) {
  asm volatile("s_mov_b32 m0, %0\n\ts_nop 0\n\t"
               "buffer_load_dwordx4 %1, %4, %5 offen lds\n\t"
               "buffer_load_dwordx4 %2, %4, %5 offen offset:1024 lds\n\t"
               "buffer_load_dwordx4 %3, %4, %5 offen offset:2048 lds"
               :: "s"(lds), "v"(v0), "v"(v1), "v"(v2), "s"(r), "s"(so) : "memory");
}
DI void dma3w(__amdgpu_buffer_rsrc_t r, unsigned lds, unsigned v0, unsigned v1, unsigned v2, unsigned so) {
  asm volatile("s_mov_b32 m0, %0\n\ts_nop 3\n\t"
               "buffer_load_dwordx4 %1, %4, %5 offen lds\n\t"
               "buffer_load_dwordx4 %2, %4, %5 offen offset:1024 lds\n\t"
               "buffer_load_dwordx4 %3, %4, %5 offen offset:2048 lds"
               :: "s"(lds), "v"(v0), "v"(v1), "v"(v2), "s"(r), "s"(so) : "memory");
}
DI void touch(__amdgpu_buffer_rsrc_t r, unsigned lds, unsigned v, unsigned so) {
  asm volatile("s_mov_b32 m0, %0\n\ts_nop 0\n\tbuffer_load_dword %1, %2, %3 offen lds" :: "s"(lds), "v"(v), "s"(r), "s"(so) : "memory");
}

#define CODEPF 1
#define TLBPF 1
constexpr unsigned KVS = 16384, NKV = 4;
constexpr unsigned QB1 = 73728, QB2 = 118784;
constexpr unsigned SCR = 65536;
constexpr unsigned OREG = 65536, WREG = 114688, WOU = 12288;
constexpr unsigned FLG = 2048;
constexpr unsigned PART = 110592;

__global__ __launch_bounds__(512, 2) void attn_main(const char* xA, const char* kA, const char* vA, const char* wqA, const char* woA,
                                                    const char* xB, const char* kB, const char* vB, const char* wqB, const char* woB, char* ows, char* out) {
  extern __shared__ __attribute__((aligned(16))) char lds[];
  const unsigned ldsb = rfl((unsigned)(uintptr_t)(LAS char*)lds);
  const unsigned wid = rfl(threadIdx.x >> 6), lane = threadIdx.x & 63, team = wid >> 2, u = wid & 3, jj = u >> 1, qh = u & 1;
  const unsigned id = blockIdx.x, xcd = id & 7, idx = id >> 3, blk = xcd >> 2;
  const unsigned b = idx & 15, hb = 8 * (xcd & 3) + 4 * (idx >> 4), h = hb + 2 * team + jj;
  const char* kp = blk ? kB : kA; const char* vp = blk ? vB : vA; const char* wop = blk ? woB : woA;
  const __amdgpu_buffer_rsrc_t rk = mkrsrc(kp + (size_t)(b * NH + hb) * SK * DH * 2, 4 * SK * DH * 2);
  const __amdgpu_buffer_rsrc_t rv = mkrsrc(vp + (size_t)(b * NH + hb) * SK * DH * 2, 4 * SK * DH * 2);
  const __amdgpu_buffer_rsrc_t ro = mkrsrc(wop, HD * DM * 2);
  const unsigned grp = blk * NB + b, mem = hb >> 2;
  const __amdgpu_buffer_rsrc_t rf = mkrsrc(ows + (size_t)256 * PART + grp * FLG, FLG);
  const unsigned g = lane >> 4, c16 = lane & 15, q4 = c16 >> 2, p4 = lane & 3;
  const unsigned vka = g * 256 + 16 * (c16 ^ ((8 * qh) | g)), vkb = g * 256 + 16 * (c16 ^ ((8 * qh + 4) | g));
  const unsigned vva = g * 256 + 16 * (c16 ^ (g << 2)), vvb = g * 256 + 16 * (c16 ^ ((g << 2) | 2));
  const unsigned krot = (id * 5 + (id >> 3) + jj * 7) & 15;
  const unsigned kvs0 = jj * (SK * DH * 2) + 4096 * qh, kvd0 = ldsb + 8192 * jj + 4096 * qh;
  auto kv_so = [&](unsigned T) { return kvs0 + ((T & 16) << 14) + (((T + krot) & 15) << 13); };
  auto k_issue = [&](unsigned T) { dma4_nt(rk, kvd0 + 3 * KVS - (T & 1) * (2 * KVS), vka, vkb, kv_so(T)); };
  auto v_issue = [&](unsigned T) { dma4_nt(rv, kvd0 + (T & 1) * (2 * KVS), vva, vvb, kv_so(T)); };
  const unsigned tro = 256 * (8 * g + q4) + 16 * ((q4 << 2) | ((g & 1) << 1) | (p4 >> 1)) + 8 * (p4 & 1);
  const unsigned xo0 = 32768 + (48 * qh + c16) * 128 + 16 * (g ^ ((c16 >> 1) & 7));
  auto rd_tr8 = [&](bf16x8 (&f)[8], unsigned a, unsigned imm) {
#pragma unroll
    for (int d = 0; d < 8; ++d) f[d] = lds_tr2((a ^ (32 * d)) + imm, (a ^ (32 * d)) + imm + 1024);
  };

  bf16x8 qf[4][3];
  f32x4 acc[8][3];
  auto zero_acc = [&]() {
#pragma unroll
    for (int d = 0; d < 8; ++d)
#pragma unroll
      for (int q = 0; q < 3; ++q) acc[d][q] = f32x4{};
  };
  auto mm24 = [&](const bf16x8 (&a)[8], const bf16x8 (&x)[3]) {
#pragma unroll
    for (int d = 0; d < 8; ++d)
#pragma unroll
      for (int q = 0; q < 3; ++q) acc[d][q] = MFMA16(a[d], x[q], acc[d][q]);
  };
  auto pack_q = [&]() {
#pragma unroll
    for (int s = 0; s < 4; ++s)
#pragma unroll
      for (int q = 0; q < 3; ++q) qf[s][q] = pack_swap(acc[2 * s][q] * SC_LOG2E, acc[2 * s + 1][q] * SC_LOG2E);
  };

  unsigned long long pc0; unsigned clen;
  asm volatile("attn_code_begin:\n\ts_getpc_b64 %0\n\ts_mov_b32 %1, attn_code_end-attn_code_begin-8" : "=s"(pc0), "=s"(clen));
  if (team == 0) {
    if (u == 1) touch(mkrsrc((const void*)pc0, clen), ldsb + SCR + 256, (idx * 8 + (lane & 7)) * 128, 0);
    if (u == 2) { touch(rk, ldsb + SCR + 512, (lane & 3) * 131072, 0); touch(rv, ldsb + SCR + 512, (lane & 3) * 131072, 0); }
    if (u == 3) { touch(ro, ldsb + SCR + 512, 0, hb * DH * (DM * 2)); touch(mkrsrc(ows + (size_t)grp * 8 * PART, 8 * PART), ldsb + SCR + 512, (lane & 7) * PART, 0); touch(mkrsrc(out + (size_t)b * SQ * 2 * DM * 2, 4096), ldsb + SCR + 512, 0, 0); }
    if (u == 0) {
      const unsigned old = __builtin_amdgcn_raw_buffer_load_b32(rf, lane < 24 ? 128 * (1 + mem) + 4 * lane : 4 * mem, 0, 16);
      const unsigned base = __builtin_amdgcn_readlane(old, 24) + 1;
      unsigned k = 0;
      while (__any(old - base == k)) ++k;
      if (lane == 0) __builtin_amdgcn_raw_buffer_store_b32(base + k, rf, 4 * mem, 0, 16);
    }
    zero_acc();
    bf16x8 wa[8], wb[8], xa[3], xb[3];
#pragma unroll
    for (int d = 0; d < 8; ++d) wb[d] = bf16x8{};
#pragma unroll
    for (int q = 0; q < 3; ++q) xb[q] = bf16x8{};
    unsigned bufb = ldsb;
#pragma clang loop unroll(disable)
    for (int c = 0; c < 9; ++c) {
      const unsigned a = bufb + 16384 * jj + tro, xo = bufb + xo0;
      bufb = bufb == ldsb ? ldsb + QB1 : bufb == ldsb + QB1 ? ldsb + QB2 : ldsb;
      BARRIER();
#pragma unroll
      for (int q = 0; q < 3; ++q) xa[q] = lds_b128(xo + 2048 * q);
      rd_tr8(wa, a, 0);
      mm24(wb, xb);
      weave<19>();
      SB0();
#pragma unroll
      for (int q = 0; q < 3; ++q) xb[q] = lds_b128((xo ^ 64) + 2048 * q);
      rd_tr8(wb, a, 8192);
      mm24(wa, xa);
      weave<19>();
    }
    mm24(wb, xb);
    pack_q();
  } else {
    const char* xp = blk ? xB : xA; const char* wp = blk ? wqB : wqA;
    const __amdgpu_buffer_rsrc_t rx = mkrsrc(xp + (size_t)b * SQ * DM * 2, SQ * DM * 2);
    const __amdgpu_buffer_rsrc_t rw = mkrsrc(wp, DM * HD * 2);
    unsigned vw[4], vx[3];
#pragma unroll
    for (int pp = 0; pp < 4; ++pp) vw[pp] = (4 * pp + g) * (HD * 2) - 1024 * pp + 16 * (c16 ^ ((g << 2) | (pp & 2)));
#pragma unroll
    for (int i = 0; i < 3; ++i) vx[i] = (8 * i + (lane >> 3)) * (DM * 2) - 1024 * i + 16 * ((lane & 7) ^ ((4 * (3 * u + i) + g) & 7));
    const unsigned qrot = (b + 4 * (idx >> 4)) % 9;
    auto x_issue = [&](unsigned c, unsigned buf) { dma3(rx, buf + 32768 + 3072 * u, vx[0], vx[1], vx[2], c * 128 + 24 * u * (DM * 2)); };
    auto w_issue = [&](unsigned c, unsigned ks, unsigned buf, unsigned head) {
      dma4(rw, buf + 16384 * jj + 8192 * ks + 4096 * qh, vw[0], vw[1], vw[2], vw[3], (c * 64 + 32 * ks + 16 * qh) * (HD * 2) + head * 256);
    };
    auto nxt = [&](unsigned c) { return c == 8 ? 0u : c + 1; };
    unsigned cq = qrot;
    x_issue(cq, ldsb); w_issue(cq, 0, ldsb, h - 2); w_issue(cq, 1, ldsb, h - 2); cq = nxt(cq);
    x_issue(cq, ldsb + QB1); w_issue(cq, 0, ldsb + QB1, h - 2); w_issue(cq, 1, ldsb + QB1, h - 2); cq = nxt(cq);
    { unsigned c = 2 + (lane >= 24) + (lane >= 48) + qrot; if (c >= 9) c -= 9;
      touch(rx, ldsb + SCR, (24 * u + lane % 24) * (DM * 2) + c * 128, 0);
      c = 4 + qrot; if (c >= 9) c -= 9;
      touch(rx, ldsb + SCR, (24 * u + 16 + (lane & 7)) * (DM * 2) + c * 128, 0); }
    unsigned bufb = ldsb + QB2;
    WAITV(13);
#pragma clang loop unroll(disable)
    for (int c = 0; c < 7; ++c) {
      BARRIER();
      x_issue(cq, bufb); w_issue(cq, 0, bufb, h - 2); w_issue(cq, 1, bufb, h - 2); cq = nxt(cq);
      bufb = bufb == ldsb ? ldsb + QB1 : bufb == ldsb + QB1 ? ldsb + QB2 : ldsb;
      WAITV(11);
    }
    k_issue(0);
    BARRIER();
    v_issue(0);
    WAITV(8); BARRIER();
    k_issue(1); v_issue(1);
    zero_acc();
    WAITV(12); BARRIER();
    x_issue(cq, ldsb + QB1); w_issue(cq, 0, ldsb + QB1, h); w_issue(cq, 1, ldsb + QB1, h); cq = nxt(cq);
    WAITV(19); BARRIER();
    k_issue(2);
    unsigned T = 1;
#pragma clang loop unroll(disable)
    for (int c = 0; c < 9; ++c) {
      const unsigned qb = ldsb + ((c & 1) ? QB2 : QB1), qn = ldsb + ((c & 1) ? QB1 : QB2);
      const bool fast = c & 1;
      bf16x8 wf[2][8], xf[2][3];
      if (c == 0) WAITV(4); else WAITV(8);
      BARRIER();
#pragma unroll
      for (int q = 0; q < 3; ++q) xf[0][q] = lds_b128(qb + xo0 + 2048 * q);
      rd_tr8(wf[0], qb + 16384 * jj + tro, 0);
      if (!fast) {
        if (c < 8) { x_issue(cq, qn); w_issue(cq, 0, qn, h); }
        v_issue(T + 1);
        if (c < 8) WAITV(15); else WAITV(8);
        BARRIER();
        k_issue(T + 2); ++T;
        mm24(wf[0], xf[0]);
        if (c < 8) WAITV(15); else WAITV(8);
        BARRIER();
      } else { x_issue(cq, qn); w_issue(cq, 0, qn, h); }
#pragma unroll
      for (int q = 0; q < 3; ++q) xf[1][q] = lds_b128(((qb + xo0) ^ 64) + 2048 * q);
      rd_tr8(wf[1], qb + 16384 * jj + tro, 8192);
      if (c < 8) w_issue(cq, 1, qn, h);
      cq = nxt(cq);
      v_issue(T + 1);
      if (fast) WAITV(19); else if (c < 8) WAITV(12); else WAITV(8);
      BARRIER();
      k_issue(T + 2); ++T;
      if (fast) mm24(wf[0], xf[0]);
      mm24(wf[1], xf[1]);
    }
    WAITV(8); BARRIER();
    v_issue(16);
    pack_q();
    WAITV(8); BARRIER();
    k_issue(17);
  }

  const unsigned prot = (b >> 3) + (b & 1), ksrot = b & 7;
  unsigned vo[3];
#pragma unroll
  for (int i = 0; i < 3; ++i) { const unsigned L = 64 * i + lane, row = L / 24, ph = L - 24 * row; vo[i] = row * (DM * 2) + 16 * (ph ^ (row & 6)) - 1024 * i; }
  unsigned wo_tab;
  { const unsigned r = lane >= 24, k = lane - 24 * r; unsigned p = ((k >> 3) + prot + 2 * r) % 3;
    wo_tab = ((hb + 2 * r) * DH + 32 * ((k + ksrot) & 7) + 8 * u) * (DM * 2) + 384 * p; }
  auto wo_issue = [&](unsigned i, unsigned buf) { dma3w(ro, buf + 3072 * u, vo[0], vo[1], vo[2], __builtin_amdgcn_readlane(wo_tab, i)); };

  {
    zero_acc();
    f32x4 negm[3] = {f32x4{}, f32x4{}, f32x4{}};
    f32x2 l2[3] = {f32x2{}, f32x2{}, f32x2{}};
    const unsigned koff = ldsb + KVS + 8192 * jj + 256 * (8 * q4 + p4) + 16 * (g ^ c16), voff = ldsb + 8192 * jj + tro;
    f32x4 sc[2][3];
    bf16x8 kf[2][4], vf[8], pf[3];
    auto qk = [&]() {
#pragma unroll
      for (int kb = 0; kb < 2; ++kb)
#pragma unroll
        for (int q = 0; q < 3; ++q) sc[kb][q] = MFMA16(kf[kb][0], qf[0][q], negm[q]);
#pragma unroll
      for (int s = 1; s < 4; ++s)
#pragma unroll
        for (int kb = 0; kb < 2; ++kb)
#pragma unroll
          for (int q = 0; q < 3; ++q) sc[kb][q] = MFMA16(kf[kb][s], qf[s][q], sc[kb][q]);
    };
    WAITV(4);
    BARRIER();
#pragma unroll
    for (int s = 0; s < 4; ++s) { const unsigned a = (2 * KVS + koff) ^ (64 * s); kf[0][s] = lds_b128(a); kf[1][s] = lds_b128(a + 1024); }
    qk();
#pragma clang loop unroll(disable)
    for (int t = 0; t < 16; ++t) {
      const unsigned sb = (t & 1) * (2 * KVS);
      BARRIER();
      rd_tr8(vf, sb + voff, 0);
      MEMFENCE(); SB0();
      float mx[3];
#pragma unroll
      for (int q = 0; q < 3; ++q)
        mx[q] = max3f(max3f(sc[0][q][0], sc[0][q][1], sc[0][q][2]), max3f(sc[0][q][3], sc[1][q][0], sc[1][q][1]), max2f(sc[1][q][2], sc[1][q][3]));
      if (t == 0 || __builtin_expect(__any(!(max3f(mx[0], mx[1], mx[2]) <= THR)), 0)) {
#pragma unroll
        for (int q = 0; q < 3; ++q) {
          float dl = xg_max(mx[q]);
          if (t != 0) dl = max2f(dl, 0.f);
          negm[q] -= dl; sc[0][q] -= dl; sc[1][q] -= dl; mx[q] = dl;
        }
        if (t != 0) {
#pragma unroll
          for (int q = 0; q < 3; ++q) {
            const float al = __builtin_amdgcn_exp2f(-mx[q]);
            l2[q] *= al;
#pragma unroll
            for (int d = 0; d < 8; ++d) acc[d][q] *= al;
          }
        }
      }
#pragma unroll
      for (int q = 0; q < 3; ++q) {
#pragma unroll
        for (int i = 0; i < 4; ++i) { sc[0][q][i] = __builtin_amdgcn_exp2f(sc[0][q][i]); sc[1][q][i] = __builtin_amdgcn_exp2f(sc[1][q][i]); }
        { const f32x4 s4 = sc[0][q] + sc[1][q]; l2[q] += f32x2{s4[0], s4[1]} + f32x2{s4[2], s4[3]}; }
        pf[q] = pack2(sc[0][q], sc[1][q]);
      }
      asm volatile("" : "+v"(pf[0]), "+v"(pf[1]), "+v"(pf[2]), "+v"(l2[0]), "+v"(l2[1]), "+v"(l2[2]));
      WAITV(0); BARRIER();
      if (t == 15 && team == 0) { wo_issue(0, ldsb + WREG); wo_issue(1, ldsb + WREG + WOU); v_issue(17); }
#pragma unroll
      for (int d = 0; d < 8; ++d) {
#pragma unroll
        for (int q = 0; q < 3; ++q) acc[d][q] = MFMA16(vf[d], pf[q], acc[d][q]);
        if (d >= 2 && d < 6) { const unsigned a = (sb + koff) ^ (64 * (d - 2)); kf[0][d - 2] = lds_b128(a); kf[1][d - 2] = lds_b128(a + 1024); MEMFENCE(); }
        SB0();
      }
      if (t < 15) qk();
    }
    if (team == 0) { BARRIER(); wo_issue(2, ldsb + WREG + 2 * WOU); k_issue(18); }
#pragma unroll
    for (int q = 0; q < 3; ++q) {
      const float inv = __builtin_amdgcn_rcpf(xg_sum(l2[q][0] + l2[q][1]));
#pragma unroll
      for (int s = 0; s < 4; ++s)
        *(LAS bf16x8*)(uintptr_t)(ldsb + OREG + (((4 * jj + s - ksrot) & 7) * 6 + 3 * qh + q) * 1024 + lane * 16) = pack2(acc[2 * s][q] * inv, acc[2 * s + 1][q] * inv);
    }
  }

  const __amdgpu_buffer_rsrc_t rg = mkrsrc(ows + (size_t)grp * 8 * PART, 8 * PART);
  auto await_pass = [&](unsigned pi, unsigned mine) {
#pragma clang loop unroll(disable)
    for (unsigned spin = 0; spin < (1u << 22); ++spin) {
      const unsigned ec = __builtin_amdgcn_raw_buffer_load_b32(rf, 128 * (1 + mem) + 4 * (lane & 7), 32 * pi, 16);
      MEMFENCE();
      if (__all(ec == mine)) break;
      __builtin_amdgcn_s_sleep(1);
    }
  };
  auto sum_pass = [&](auto NS_, unsigned NL, unsigned pi, unsigned r1) {
    constexpr int NS = NS_.value;
    const unsigned p = (pi + prot + 2) % 3;
    if (lane < NL) {
      const unsigned r0 = mem * 288 + r1 + lane;
      u32x4 v[NS][8];
#pragma unroll
      for (int s = 0; s < NS; ++s)
#pragma unroll
        for (int i = 0; i < 8; ++i) v[s][i] = __builtin_amdgcn_raw_buffer_load_b128(rg, (r0 + NL * s) * 16, i * PART + p * 36864, 16);
#pragma unroll
      for (int s = 0; s < NS; ++s) {
        float a[8] = {0.f, 0.f, 0.f, 0.f, 0.f, 0.f, 0.f, 0.f};
#pragma unroll
        for (int i = 0; i < 8; ++i)
#pragma unroll
          for (int k = 0; k < 4; ++k) { a[2 * k] += __uint_as_float(v[s][i][k] << 16); a[2 * k + 1] += __uint_as_float(v[s][i][k] & 0xffff0000u); }
        const unsigned r = r0 + NL * s, ln = r & 63, f = r >> 6, uu = f / 9, i9 = f - 9 * uu, np = i9 / 3, q = i9 - 3 * np;
        const unsigned row = 48 * (uu & 1) + 16 * q + (ln & 15), col = 192 * p + 96 * (uu >> 1) + 32 * np + 4 * (ln >> 4);
        char* op = out + ((size_t)(b * SQ + row) * (2 * DM) + blk * DM + col) * 2;
        *(u32x2*)op = u32x2{cvtpk(a[0], a[1]), cvtpk(a[2], a[3])};
        *(u32x2*)(op + 32) = u32x2{cvtpk(a[4], a[5]), cvtpk(a[6], a[7])};
      }
    }
  };

  if (team == 0) {
    unsigned wof[6];
#pragma unroll
    for (int n = 0; n < 6; ++n) wof[n] = ldsb + (4 * g + q4) * 384 + 16 * ((12 * jj + 2 * n + (p4 >> 1)) ^ ((4 * g + q4) & 6)) + 8 * (p4 & 1);
    const unsigned oof = ldsb + OREG + 3 * qh * 1024 + lane * 16;
    f32x4 oc[6][3];
    unsigned pA[36], pB[36];
#pragma unroll
    for (int i = 0; i < 36; ++i) { pA[i] = 0; pB[i] = 0; }
    auto mm18 = [&](const bf16x8 (&w)[6], const bf16x8 (&o)[3]) {
#pragma unroll
      for (int n = 0; n < 6; ++n)
#pragma unroll
        for (int q = 0; q < 3; ++q) oc[n][q] = MFMA16(w[n], o[q], oc[n][q]);
    };
    unsigned T = 17;
#pragma clang loop unroll(disable)
    for (int p = 0; p < 3; ++p) {
#pragma unroll
      for (int n = 0; n < 6; ++n)
#pragma unroll
        for (int q = 0; q < 3; ++q) oc[n][q] = f32x4{};
#pragma clang loop unroll(disable)
      for (unsigned k = 0; k < 8; k += 2, ++T) {
        WAITV(11); BARRIER();
        bf16x8 of[2][3], wf[2][6];
        const unsigned wb = WREG + (k & 2) * WOU, ob = oof + k * 6144;
#pragma unroll
        for (int i = 0; i < 2; ++i) {
#pragma unroll
          for (int q = 0; q < 3; ++q) of[i][q] = lds_b128(ob + 6144 * i + 1024 * q);
#pragma unroll
          for (int n = 0; n < 6; ++n) wf[i][n] = lds_tr2(wb + wof[n] + WOU * i, wb + wof[n] + WOU * i + 16 * 384);
        }
        wo_issue(2 * T - 31, ldsb + WREG + ((k & 2) ^ 3) * WOU);
        v_issue(T + 1);
        WAITV(14); BARRIER();
        wo_issue(2 * T - 30, ldsb + wb);
        k_issue(T + 2);
        mm18(wf[0], of[0]); mm18(wf[1], of[1]);
      }
      if (p == 0) {
#pragma unroll
        for (int i = 0; i < 36; ++i) { const f32x4 v = oc[i / 6][(i / 2) % 3]; pA[i] = cvtpk(v[2 * (i & 1)], v[2 * (i & 1) + 1]); }
      } else if (p == 1) {
#pragma unroll
        for (int i = 0; i < 36; ++i) { const f32x4 v = oc[i / 6][(i / 2) % 3]; pB[i] = cvtpk(v[2 * (i & 1)], v[2 * (i & 1) + 1]); }
      }
    }
    WAITV(14); BARRIER(); wo_issue(27, ldsb + WREG + 3 * WOU); v_issue(30);
    WAITV(14); BARRIER(); k_issue(31);
    WAITV(11); BARRIER(); v_issue(31);
    auto unpark = [&](const unsigned (&p)[36]) {
#pragma unroll
      for (int i = 0; i < 18; ++i) {
        unsigned v0 = p[2 * i], v1 = p[2 * i + 1]; asm volatile("" : "+v"(v0), "+v"(v1));
        oc[i / 3][i % 3] = f32x4{__uint_as_float(v0 << 16), __uint_as_float(v0 & 0xffff0000u), __uint_as_float(v1 << 16), __uint_as_float(v1 & 0xffff0000u)};
      }
    };
    WAITV(8); BARRIER();
    unsigned woh[6];
#pragma unroll
    for (int n = 0; n < 6; ++n) { woh[n] = wof[n] + WREG; asm volatile("" : "+v"(woh[n])); }
    bf16x8 wa[6], wb[6], oa[3], ob[3];
    WAITV(4); BARRIER();
    WAITV(0); BARRIER();
    wo_issue(28, ldsb); wo_issue(29, ldsb + WOU);
    BARRIER();
    wo_issue(30, ldsb + 2 * WOU); wo_issue(31, ldsb + 3 * WOU);
    const __amdgpu_buffer_rsrc_t rp = mkrsrc(ows + (size_t)(grp * 8 + mem) * PART, PART);
    unsigned tick = __builtin_amdgcn_raw_buffer_load_b32(rf, 4 * (lane < 2 ? 2 * u + lane : mem), 0, 16);
    auto publish = [&](unsigned pi) { __builtin_amdgcn_raw_buffer_store_b32(tick, rf, lane < 2 ? 128 * (1 + 2 * u + lane) + 4 * mem : FLG, 32 * pi, 16); };
    auto store_pass = [&](unsigned pi) {
      const unsigned p = (pi + prot + 2) % 3;
#pragma unroll
      for (int n = 0; n < 3; ++n)
#pragma unroll
        for (int q = 0; q < 3; ++q) __builtin_amdgcn_raw_buffer_store_b128(__builtin_bit_cast(u32x4, pack2(oc[2 * n][q], oc[2 * n + 1][q])), rp, lane * 16, p * 36864 + u * 9216 + (n * 3 + q) * 1024, 16);
    };
    auto rd_unit = [&](bf16x8 (&w)[6], bf16x8 (&o)[3], const int k) {
#pragma unroll
      for (int q = 0; q < 3; ++q) o[q] = lds_b128(oof + k * 6144 + 1024 * q);
#pragma unroll
      for (int n = 0; n < 6; ++n) { const unsigned a = (k < 4 ? woh[n] : wof[n]) + (k & 3) * WOU; w[n] = lds_tr2(a, a + 16 * 384); }
    };
    BARRIER();
    rd_unit(wa, oa, 0);
#pragma clang loop unroll(disable)
    for (unsigned p = 0; p < 3; ++p) {
#pragma unroll
      for (int j = 0; j < 4; ++j) {
        rd_unit(wb, ob, 2 * j + 1);
        mm18(wa, oa);
        weave<15>();
        MEMFENCE(); SB0();
        if (j == 1) { WAITV(0); publish(p > 0 ? p - 1 : 64); }
        if (j < 2) asm volatile("s_barrier" ::: "memory");
        else asm volatile("s_cmp_eq_u32 %0, 2\n\ts_cbranch_scc1 1f\n\ts_barrier\n1:" :: "s"(p) : "scc", "memory");
        SB0();
        rd_unit(wa, oa, (2 * j + 2) & 7);
        mm18(wb, ob);
        weave<15>();
        MEMFENCE(); SB0();
      }
      store_pass(p);
      if (p == 0) unpark(pA); else unpark(pB);
    }
    WAITV(0);
    publish(2);
    await_pass(2, __builtin_amdgcn_readlane(tick, 2));
    const unsigned now = __builtin_amdgcn_raw_buffer_load_b32(rf, 4 * (lane < 2 ? 2 * u + lane : mem), 0, 16);
    sum_pass(std::integral_constant<int, 2>{}, 36, 2, 72 * u);
    if (now != tick) { tick = now; publish(0); publish(1); publish(2); }
  } else {
    const unsigned mine = __builtin_amdgcn_raw_buffer_load_b32(rf, 4 * mem, 0, 16);
#pragma clang loop unroll(disable)
    for (unsigned j = 0; j < 11; ++j) {
      if (j < 10) WAITV(9); else WAITV(0);
      BARRIER();
      const unsigned k = 2 * j + 5;
      if (j >= 2 && j < 10) wo_issue(24 + k, ldsb + ((k & 4) ? 0 : WREG) + (k & 3) * WOU);
      if (j >= 1 && j < 9) wo_issue(25 + k, ldsb + (((k + 1) & 4) ? 0 : WREG) + ((k + 1) & 3) * WOU);
    }
    await_pass(jj, mine);
    sum_pass(std::integral_constant<int, 3>{}, 48, jj, 144 * qh);
  }
  asm volatile("s_waitcnt vmcnt(0)\nattn_code_end:" ::: "memory");
}

extern "C" void kernel_launch(void* const* d_in, const int* in_sizes, int n_in, void* d_out, int out_size, void* d_ws, size_t ws_size,
                              hipStream_t stream) {
  (void)in_sizes; (void)n_in; (void)out_size; (void)ws_size;
  (void)hipFuncSetAttribute((const void*)attn_main, hipFuncAttributeMaxDynamicSharedMemorySize, LDS_MAIN);
  attn_main<<<256, 512, LDS_MAIN, stream>>>((const char*)d_in[0], (const char*)d_in[1], (const char*)d_in[2], (const char*)d_in[3], (const char*)d_in[4],
                                            (const char*)d_in[5], (const char*)d_in[6], (const char*)d_in[7], (const char*)d_in[8], (const char*)d_in[9], (char*)d_ws, (char*)d_out);
}
```

```cpp
#include <hip/hip_runtime.h>
#include <hip/hip_bf16.h>
#include <stdint.h>
#include <type_traits>

#define LAS __attribute__((address_space(3)))
#define DI __device__ __forceinline__
typedef __bf16 bf16x2 __attribute__((ext_vector_type(2)));
typedef __bf16 bf16x4 __attribute__((ext_vector_type(4)));
typedef __bf16 bf16x8 __attribute__((ext_vector_type(8)));
typedef float f32x2 __attribute__((ext_vector_type(2)));
typedef float f32x4 __attribute__((ext_vector_type(4)));
typedef unsigned u32x4 __attribute__((ext_vector_type(4)));
typedef unsigned u32x2 __attribute__((ext_vector_type(2)));

constexpr int NB = 16, SQ = 96, DM = 576, NH = 32, SK = 512, DH = 128, HD = NH * DH;
constexpr unsigned LDS_MAIN = 163840;
constexpr float SC_LOG2E = 0.08838834764831845f * 1.4426950408889634f;
constexpr float THR = 8.0f;

DI unsigned rfl(unsigned x) { return __builtin_amdgcn_readfirstlane(x); }
DI unsigned cvtpk(float lo, float hi) { f32x2 v = {lo, hi}; bf16x2 b = __builtin_convertvector(v, bf16x2); return __builtin_bit_cast(unsigned, b); }
DI bf16x8 pack2(const f32x4& a, const f32x4& b) {
  u32x4 w = {cvtpk(a[0], a[1]), cvtpk(a[2], a[3]), cvtpk(b[0], b[1]), cvtpk(b[2], b[3])};
  return __builtin_bit_cast(bf16x8, w);
}
DI bf16x8 pack_swap(const f32x4& a0, const f32x4& a1) {
  unsigned p00 = cvtpk(a0[0], a0[1]), p01 = cvtpk(a0[2], a0[3]), p10 = cvtpk(a1[0], a1[1]), p11 = cvtpk(a1[2], a1[3]);
  auto r0 = __builtin_amdgcn_permlane32_swap(p00, p10, false, false);
  auto r1 = __builtin_amdgcn_permlane32_swap(p01, p11, false, false);
  auto t0 = __builtin_amdgcn_permlane16_swap(r0[0], r0[1], false, false);
  auto t1 = __builtin_amdgcn_permlane16_swap(r1[0], r1[1], false, false);
  u32x4 w = {t0[0], t1[0], t0[1], t1[1]};
  return __builtin_bit_cast(bf16x8, w);
}
DI bf16x8 lds_b128(unsigned a) { return *(const LAS bf16x8*)(uintptr_t)a; }
DI bf16x8 lds_tr2(unsigned a0, unsigned a1) {
  bf16x4 lo = __builtin_amdgcn_ds_read_tr16_b64_v4bf16((LAS bf16x4*)(uintptr_t)a0);
  bf16x4 hi = __builtin_amdgcn_ds_read_tr16_b64_v4bf16((LAS bf16x4*)(uintptr_t)a1);
  return __builtin_shufflevector(lo, hi, 0, 1, 2, 3, 4, 5, 6, 7);
}
DI float max2f(float a, float b) { return __builtin_elementwise_maximum(a, b); }
DI float max3f(float a, float b, float c) { return max2f(max2f(a, b), c); }
DI float xg_max(float v) {
  auto r = __builtin_amdgcn_permlane32_swap(__float_as_uint(v), __float_as_uint(v), false, false); v = max2f(__uint_as_float(r[0]), __uint_as_float(r[1]));
  auto s = __builtin_amdgcn_permlane16_swap(__float_as_uint(v), __float_as_uint(v), false, false); return max2f(__uint_as_float(s[0]), __uint_as_float(s[1]));
}
DI float xg_sum(float v) {
  auto r = __builtin_amdgcn_permlane32_swap(__float_as_uint(v), __float_as_uint(v), false, false); v = __uint_as_float(r[0]) + __uint_as_float(r[1]);
  auto s = __builtin_amdgcn_permlane16_swap(__float_as_uint(v), __float_as_uint(v), false, false); return __uint_as_float(s[0]) + __uint_as_float(s[1]);
}
#define MFMA16(a, b, c) __builtin_amdgcn_mfma_f32_16x16x32_bf16((a), (b), (c), 0, 0, 0)
#define SB0() __builtin_amdgcn_sched_barrier(0)
#define MEMFENCE() asm volatile("" ::: "memory")
#define BARRIER() do { SB0(); __builtin_amdgcn_s_waitcnt(0xC07F); asm volatile("s_barrier" ::: "memory"); SB0(); } while (0)
#define WAITV(n) asm volatile("s_waitcnt vmcnt(" #n ")" ::: "memory")
template <int N> DI void weave() {
#pragma unroll
  for (int i = 0; i < N; ++i) { __builtin_amdgcn_sched_group_barrier(0x8, 1, 0); __builtin_amdgcn_sched_group_barrier(0x100, 1, 0); }
}
template <int N> DI void weave2() {
#pragma unroll
  for (int i = 0; i < N; ++i) { __builtin_amdgcn_sched_group_barrier(0x8, 2, 0); __builtin_amdgcn_sched_group_barrier(0x100, 2, 0); }
}
DI __amdgpu_buffer_rsrc_t mkrsrc(const void* p, unsigned bytes) { return __builtin_amdgcn_make_buffer_rsrc((void*)p, (short)0, (int)bytes, 0x00020000); }
DI void dma4_nt(__amdgpu_buffer_rsrc_t r, unsigned lds, unsigned va, unsigned vb, unsigned so) {
  asm volatile("s_mov_b32 m0, %0\n\ts_nop 0\n\t"
               "buffer_load_dwordx4 %1, %3, %4 offen nt lds\n\t"
               "buffer_load_dwordx4 %1, %3, %4 offen offset:1024 nt lds\n\t"
               "buffer_load_dwordx4 %2, %3, %4 offen offset:2048 nt lds\n\t"
               "buffer_load_dwordx4 %2, %3, %4 offen offset:3072 nt lds"
               :: "s"(lds), "v"(va), "v"(vb), "s"(r), "s"(so) : "memory");
}
DI void dma4(__amdgpu_buffer_rsrc_t r, unsigned lds, unsigned v0, unsigned v1, unsigned v2, unsigned v3, unsigned so) {
  asm volatile("s_mov_b32 m0, %0\n\ts_nop 0\n\t"
               "buffer_load_dwordx4 %1, %5, %6 offen lds\n\t"
               "buffer_load_dwordx4 %2, %5, %6 offen offset:1024 lds\n\t"
               "buffer_load_dwordx4 %3, %5, %6 offen offset:2048 lds\n\t"
               "buffer_load_dwordx4 %4, %5, %6 offen offset:3072 lds"
               :: "s"(lds), "v"(v0), "v"(v1), "v"(v2), "v"(v3), "s"(r), "s"(so) : "memory");
}
DI void dma3(__amdgpu_buffer_rsrc_t r, unsigned lds, unsigned v0, unsigned v1, unsigned v2, unsigned so) {
  asm volatile("s_mov_b32 m0, %0\n\ts_nop 0\n\t"
               "buffer_load_dwordx4 %1, %4, %5 offen lds\n\t"
               "buffer_load_dwordx4 %2, %4, %5 offen offset:1024 lds\n\t"
               "buffer_load_dwordx4 %3, %4, %5 offen offset:2048 lds"
               :: "s"(lds), "v"(v0), "v"(v1), "v"(v2), "s"(r), "s"(so) : "memory");
}
DI void dma3w(__amdgpu_buffer_rsrc_t r, unsigned lds, unsigned v0, unsigned v1, unsigned v2, unsigned so) {
  asm volatile("s_mov_b32 m0, %0\n\ts_nop 3\n\t"
               "buffer_load_dwordx4 %1, %4, %5 offen lds\n\t"
               "buffer_load_dwordx4 %2, %4, %5 offen offset:1024 lds\n\t"
               "buffer_load_dwordx4 %3, %4, %5 offen offset:2048 lds"
               :: "s"(lds), "v"(v0), "v"(v1), "v"(v2), "s"(r), "s"(so) : "memory");
}
DI void touch(__amdgpu_buffer_rsrc_t r, unsigned lds, unsigned v, unsigned so) {
  asm volatile("s_mov_b32 m0, %0\n\ts_nop 0\n\tbuffer_load_dword %1, %2, %3 offen lds" :: "s"(lds), "v"(v), "s"(r), "s"(so) : "memory");
}

#define KVREC (4 * SK * DH * 2)
#define CODEPF 1
#define TLBPF 1
constexpr unsigned KVS = 16384, NKV = 4;
constexpr unsigned QB1 = 73728, QB2 = 118784;
constexpr unsigned SCR = 65536;
constexpr unsigned OREG = 65536, WREG = 114688, WOU = 12288;
constexpr unsigned FLG = 2048;
constexpr unsigned PART = 110592;

__global__ __launch_bounds__(512, 2) void attn_main(const char* xA, const char* kA, const char* vA, const char* wqA, const char* woA,
                                                    const char* xB, const char* kB, const char* vB, const char* wqB, const char* woB, char* ows, char* out) {
  extern __shared__ __attribute__((aligned(16))) char lds[];
  const unsigned ldsb = rfl((unsigned)(uintptr_t)(LAS char*)lds);
  const unsigned wid = rfl(threadIdx.x >> 6), lane = threadIdx.x & 63, team = wid >> 2, u = wid & 3, jj = u >> 1, qh = u & 1;
  const unsigned id = blockIdx.x, xcd = id & 7, idx = id >> 3, blk = xcd >> 2;
  const unsigned b = idx & 15, hb = 8 * (xcd & 3) + 4 * (idx >> 4), h = hb + 2 * team + jj;
  const char* kp = blk ? kB : kA; const char* vp = blk ? vB : vA; const char* wop = blk ? woB : woA;
  const __amdgpu_buffer_rsrc_t rk = mkrsrc(kp + (size_t)(b * NH + hb) * SK * DH * 2, KVREC);
  const __amdgpu_buffer_rsrc_t rv = mkrsrc(vp + (size_t)(b * NH + hb) * SK * DH * 2, KVREC);
  const __amdgpu_buffer_rsrc_t ro = mkrsrc(wop, HD * DM * 2);
  const unsigned grp = blk * NB + b, mem = hb >> 2;
  const __amdgpu_buffer_rsrc_t rf = mkrsrc(ows + (size_t)256 * PART + grp * FLG, FLG);
  const unsigned g = lane >> 4, c16 = lane & 15, q4 = c16 >> 2, p4 = lane & 3;
  const unsigned vka = g * 256 + 16 * (c16 ^ ((8 * qh) | g)), vkb = g * 256 + 16 * (c16 ^ ((8 * qh + 4) | g));
  const unsigned vva = g * 256 + 16 * (c16 ^ (g << 2)), vvb = g * 256 + 16 * (c16 ^ ((g << 2) | 2));
  const unsigned krot = (id * 5 + (id >> 3) + jj * 7) & 15;
  const unsigned kvs0 = jj * (SK * DH * 2) + 4096 * qh, kvd0 = ldsb + 8192 * jj + 4096 * qh;
  auto kv_so = [&](unsigned T) { return kvs0 + ((T & 16) << 14) + (((T + krot) & 15) << 13); };
  auto k_issue = [&](unsigned T) { dma4_nt(rk, kvd0 + 3 * KVS - (T & 1) * (2 * KVS), vka, vkb, kv_so(T)); };
  auto v_issue = [&](unsigned T) { dma4_nt(rv, kvd0 + (T & 1) * (2 * KVS), vva, vvb, kv_so(T)); };
  const unsigned tro = 256 * (8 * g + q4) + 16 * ((q4 << 2) | ((g & 1) << 1) | (p4 >> 1)) + 8 * (p4 & 1);
  const unsigned xo0 = 32768 + (48 * qh + c16) * 128 + 16 * (g ^ ((c16 >> 1) & 7));
  auto rd_tr8 = [&](bf16x8 (&f)[8], unsigned a, unsigned imm) {
#pragma unroll
    for (int d = 0; d < 8; ++d) f[d] = lds_tr2((a ^ (32 * d)) + imm, (a ^ (32 * d)) + imm + 1024);
  };

  bf16x8 qf[4][3];
  f32x4 acc[8][3];
  auto zero_acc = [&]() {
#pragma unroll
    for (int d = 0; d < 8; ++d)
#pragma unroll
      for (int q = 0; q < 3; ++q) acc[d][q] = f32x4{};
  };
  auto mm24 = [&](const int hf, const bf16x8 (&w)[8], const bf16x8 (&x)[6], auto&& fill) {
#pragma unroll
    for (int j = 0; j < 12; ++j) {
      const int i = j / 3, q = j % 3;
      acc[4 * hf + i][q] = MFMA16(w[2 * i], x[q], acc[4 * hf + i][q]); acc[4 * hf + i][q] = MFMA16(w[2 * i + 1], x[3 + q], acc[4 * hf + i][q]);
      fill(j);
      SB0();
    }
  };
  auto nofill = [](int) {};
  auto rd_w1 = [&](const int hf, const int i, unsigned a) { const unsigned p = (a ^ (128 * hf + 32 * (i >> 1))) + 8192 * (i & 1); return lds_tr2(p, p + 1024); };
  auto rd_x1 = [&](const int i, unsigned xo) { return lds_b128((xo ^ (64 * (i / 3))) + 2048 * (i % 3)); };
  auto rd_w = [&](const int hf, bf16x8 (&w)[8], unsigned a) {
#pragma unroll
    for (int i = 0; i < 8; ++i) { const unsigned p = (a ^ (128 * hf + 32 * (i >> 1))) + 8192 * (i & 1); w[i] = lds_tr2(p, p + 1024); }
  };
  auto rd_x = [&](bf16x8 (&x)[6], unsigned xo) {
#pragma unroll
    for (int i = 0; i < 6; ++i) x[i] = lds_b128((xo ^ (64 * (i / 3))) + 2048 * (i % 3));
  };
  auto pack_q = [&]() {
#pragma unroll
    for (int s = 0; s < 4; ++s)
#pragma unroll
      for (int q = 0; q < 3; ++q) qf[s][q] = pack_swap(acc[2 * s][q] * SC_LOG2E, acc[2 * s + 1][q] * SC_LOG2E);
  };

  unsigned long long pc0; unsigned clen;
  asm volatile("attn_code_begin:\n\ts_getpc_b64 %0\n\ts_mov_b32 %1, attn_code_end-attn_code_begin-8" : "=s"(pc0), "=s"(clen));
  if (team == 0) {
    if (u == 1) touch(mkrsrc((const void*)pc0, clen), ldsb + SCR + 256, (idx * 8 + (lane & 7)) * 128, 0);
    if (u == 2) { touch(rk, ldsb + SCR + 512, (lane & 3) * 131072, 0); touch(rv, ldsb + SCR + 512, (lane & 3) * 131072, 0); }
    if (u == 3) { touch(ro, ldsb + SCR + 512, 0, hb * DH * (DM * 2)); touch(mkrsrc(ows + (size_t)grp * 8 * PART, 8 * PART), ldsb + SCR + 512, (lane & 7) * PART, 0); touch(mkrsrc(out + (size_t)b * SQ * 2 * DM * 2, 4096), ldsb + SCR + 512, 0, 0); }
    if (u == 0) {
      const unsigned old = __builtin_amdgcn_raw_buffer_load_b32(rf, lane < 24 ? 128 * (1 + mem) + 4 * lane : 4 * mem, 0, 16);
      const unsigned base = __builtin_amdgcn_readlane(old, 24) + 1;
      unsigned k = 0;
      while (__any(old - base == k)) ++k;
      if (lane == 0) __builtin_amdgcn_raw_buffer_store_b32(base + k, rf, 4 * mem, 0, 16);
    }
    zero_acc();
    bf16x8 wa[8], wb[8], xa[6], xb[6];
#pragma unroll
    for (int d = 0; d < 8; ++d) wb[d] = bf16x8{};
#pragma unroll
    for (int q = 0; q < 6; ++q) xb[q] = bf16x8{};
    unsigned bufb = ldsb;
#pragma clang loop unroll(disable)
    for (int c = 0; c < 9; ++c) {
      const unsigned a = bufb + 16384 * jj + tro, xo = bufb + xo0;
      bufb = bufb == ldsb ? ldsb + QB1 : bufb == ldsb + QB1 ? ldsb + QB2 : ldsb;
      BARRIER();
      mm24(1, wb, xb, [&](int j) { if (j < 3) { xa[j] = rd_x1(j, xo); xa[j + 3] = rd_x1(j + 3, xo); } else if (j < 11) wa[j - 3] = rd_w1(0, j - 3, a); });
      mm24(0, wa, xa, [&](int j) { if (j < 8) wb[j] = rd_w1(1, j, a); });
#pragma unroll
      for (int q = 0; q < 6; ++q) xb[q] = xa[q];
    }
    mm24(1, wb, xb, nofill);
    pack_q();
  } else {
    const char* xp = blk ? xB : xA; const char* wp = blk ? wqB : wqA;
    const __amdgpu_buffer_rsrc_t rx = mkrsrc(xp + (size_t)b * SQ * DM * 2, SQ * DM * 2);
    const __amdgpu_buffer_rsrc_t rw = mkrsrc(wp, DM * HD * 2);
    unsigned vw[4], vx[3];
#pragma unroll
    for (int pp = 0; pp < 4; ++pp) vw[pp] = (4 * pp + g) * (HD * 2) - 1024 * pp + 16 * (c16 ^ ((g << 2) | (pp & 2)));
#pragma unroll
    for (int i = 0; i < 3; ++i) vx[i] = (8 * i + (lane >> 3)) * (DM * 2) - 1024 * i + 16 * ((lane & 7) ^ ((4 * (3 * u + i) + g) & 7));
    const unsigned qrot = (b + 4 * (idx >> 4)) % 9;
    auto x_issue = [&](unsigned c, unsigned buf) { dma3(rx, buf + 32768 + 3072 * u, vx[0], vx[1], vx[2], c * 128 + 24 * u * (DM * 2)); };
    auto w_issue = [&](unsigned c, unsigned ks, unsigned buf, unsigned head) {
      dma4(rw, buf + 16384 * jj + 8192 * ks + 4096 * qh, vw[0], vw[1], vw[2], vw[3], (c * 64 + 32 * ks + 16 * qh) * (HD * 2) + head * 256);
    };
    auto nxt = [&](unsigned c) { return c == 8 ? 0u : c + 1; };
    unsigned cq = qrot;
    x_issue(cq, ldsb); w_issue(cq, 0, ldsb, h - 2); w_issue(cq, 1, ldsb, h - 2); cq = nxt(cq);
    x_issue(cq, ldsb + QB1); w_issue(cq, 0, ldsb + QB1, h - 2); w_issue(cq, 1, ldsb + QB1, h - 2); cq = nxt(cq);
    { unsigned c = 2 + (lane >= 24) + (lane >= 48) + qrot; if (c >= 9) c -= 9;
      touch(rx, ldsb + SCR, (24 * u + lane % 24) * (DM * 2) + c * 128, 0);
      c = 4 + qrot; if (c >= 9) c -= 9;
      touch(rx, ldsb + SCR, (24 * u + 16 + (lane & 7)) * (DM * 2) + c * 128, 0); }
    unsigned bufb = ldsb + QB2;
    WAITV(13);
#pragma clang loop unroll(disable)
    for (int c = 0; c < 7; ++c) {
      BARRIER();
      x_issue(cq, bufb); w_issue(cq, 0, bufb, h - 2); w_issue(cq, 1, bufb, h - 2); cq = nxt(cq);
      bufb = bufb == ldsb ? ldsb + QB1 : bufb == ldsb + QB1 ? ldsb + QB2 : ldsb;
      WAITV(11);
    }
    k_issue(0);
    BARRIER();
    v_issue(0);
    WAITV(8); BARRIER();
    k_issue(1); v_issue(1);
    zero_acc();
    WAITV(12); BARRIER();
    x_issue(cq, ldsb + QB1); w_issue(cq, 0, ldsb + QB1, h); w_issue(cq, 1, ldsb + QB1, h); cq = nxt(cq);
    WAITV(19); BARRIER();
    k_issue(2);
    unsigned T = 1;
#pragma clang loop unroll(disable)
    for (int c = 0; c < 9; ++c) {
      const unsigned qb = ldsb + ((c & 1) ? QB2 : QB1), qn = ldsb + ((c & 1) ? QB1 : QB2);
      const bool fast = c & 1;
      bf16x8 wf[2][8], xf[6];
      if (c == 0) WAITV(4); else WAITV(8);
      BARRIER();
      rd_x(xf, qb + xo0);
      rd_w(0, wf[0], qb + 16384 * jj + tro);
      if (!fast) {
        if (c < 8) { x_issue(cq, qn); w_issue(cq, 0, qn, h); }
        v_issue(T + 1);
        if (c < 8) WAITV(15); else WAITV(8);
        BARRIER();
        k_issue(T + 2); ++T;
        mm24(0, wf[0], xf, nofill);
        if (c < 8) WAITV(15); else WAITV(8);
        BARRIER();
      } else { x_issue(cq, qn); w_issue(cq, 0, qn, h); }
      rd_w(1, wf[1], qb + 16384 * jj + tro);
      if (c < 8) w_issue(cq, 1, qn, h);
      cq = nxt(cq);
      v_issue(T + 1);
      if (fast) WAITV(19); else if (c < 8) WAITV(12); else WAITV(8);
      BARRIER();
      k_issue(T + 2); ++T;
      if (fast) mm24(0, wf[0], xf, nofill);
      mm24(1, wf[1], xf, nofill);
    }
    WAITV(8); BARRIER();
    v_issue(16);
    pack_q();
    WAITV(8); BARRIER();
    k_issue(17);
  }

  const unsigned prot = (b >> 3) + (b & 1), ksrot = b & 7;
  unsigned vo[3];
#pragma unroll
  for (int i = 0; i < 3; ++i) { const unsigned L = 64 * i + lane, row = L / 24, ph = L - 24 * row; vo[i] = row * (DM * 2) + 16 * (ph ^ (row & 6)) - 1024 * i; }
  unsigned wo_tab;
  { const unsigned r = lane >= 24, k = lane - 24 * r; unsigned p = ((k >> 3) + prot + 2 * r) % 3;
    wo_tab = ((hb + 2 * r) * DH + 32 * ((k + ksrot) & 7) + 8 * u) * (DM * 2) + 384 * p; }
  auto wo_issue = [&](unsigned i, unsigned buf) { dma3w(ro, buf + 3072 * u, vo[0], vo[1], vo[2], __builtin_amdgcn_readlane(wo_tab, i)); };

  {
    zero_acc();
    f32x4 negm[3] = {f32x4{}, f32x4{}, f32x4{}};
    f32x2 l2[3] = {f32x2{}, f32x2{}, f32x2{}};
    const unsigned koff = ldsb + KVS + 8192 * jj + 256 * (8 * q4 + p4) + 16 * (g ^ c16), voff = ldsb + 8192 * jj + tro;
    f32x4 sc[2][3];
    bf16x8 kf[2][4], vf[8], pf[3];
    auto qk = [&]() {
#pragma unroll
      for (int kb = 0; kb < 2; ++kb)
#pragma unroll
        for (int q = 0; q < 3; ++q) {
          sc[kb][q] = MFMA16(kf[kb][0], qf[0][q], negm[q]);
#pragma unroll
          for (int s = 1; s < 4; ++s) sc[kb][q] = MFMA16(kf[kb][s], qf[s][q], sc[kb][q]);
          SB0();
        }
    };
    WAITV(4);
    BARRIER();
#pragma unroll
    for (int s = 0; s < 4; ++s) { const unsigned a = (2 * KVS + koff) ^ (64 * s); kf[0][s] = lds_b128(a); kf[1][s] = lds_b128(a + 1024); }
    qk();
#pragma clang loop unroll(disable)
    for (int t = 0; t < 16; ++t) {
      const unsigned sb = (t & 1) * (2 * KVS);
      BARRIER();
      rd_tr8(vf, sb + voff, 0);
      MEMFENCE(); SB0();
      float mx[3];
#pragma unroll
      for (int q = 0; q < 3; ++q)
        mx[q] = max3f(max3f(sc[0][q][0], sc[0][q][1], sc[0][q][2]), max3f(sc[0][q][3], sc[1][q][0], sc[1][q][1]), max2f(sc[1][q][2], sc[1][q][3]));
      if (t == 0 || __builtin_expect(__any(!(max3f(mx[0], mx[1], mx[2]) <= THR)), 0)) {
#pragma unroll
        for (int q = 0; q < 3; ++q) {
          float dl = xg_max(mx[q]);
          if (t != 0) dl = max2f(dl, 0.f);
          negm[q] -= dl; sc[0][q] -= dl; sc[1][q] -= dl; mx[q] = dl;
        }
        if (t != 0) {
#pragma unroll
          for (int q = 0; q < 3; ++q) {
            const float al = __builtin_amdgcn_exp2f(-mx[q]);
            l2[q] *= al;
#pragma unroll
            for (int d = 0; d < 8; ++d) acc[d][q] *= al;
          }
        }
      }
#pragma unroll
      for (int q = 0; q < 3; ++q) {
#pragma unroll
        for (int i = 0; i < 4; ++i) { sc[0][q][i] = __builtin_amdgcn_exp2f(sc[0][q][i]); sc[1][q][i] = __builtin_amdgcn_exp2f(sc[1][q][i]); }
        { const f32x4 s4 = sc[0][q] + sc[1][q]; l2[q] += f32x2{s4[0], s4[1]} + f32x2{s4[2], s4[3]}; }
        pf[q] = pack2(sc[0][q], sc[1][q]);
      }
      asm volatile("" : "+v"(pf[0]), "+v"(pf[1]), "+v"(pf[2]), "+v"(l2[0]), "+v"(l2[1]), "+v"(l2[2]));
      WAITV(0); BARRIER();
      if (t == 15 && team == 0) { wo_issue(0, ldsb + WREG); wo_issue(1, ldsb + WREG + WOU); v_issue(17); }
#pragma unroll
      for (int d = 0; d < 8; ++d) {
#pragma unroll
        for (int q = 0; q < 3; ++q) acc[d][q] = MFMA16(vf[d], pf[q], acc[d][q]);
        if (d >= 2 && d < 6) { const int kb = (d - 2) >> 1, s = (d & 1) * 2; kf[kb][s] = lds_b128(((sb + koff) ^ (64 * s)) + 1024 * kb); kf[kb][s + 1] = lds_b128(((sb + koff) ^ (64 * s + 64)) + 1024 * kb); MEMFENCE(); }
        SB0();
      }
      if (t < 15) qk();
    }
    if (team == 0) { BARRIER(); wo_issue(2, ldsb + WREG + 2 * WOU); k_issue(18); }
#pragma unroll
    for (int q = 0; q < 3; ++q) {
      const float inv = __builtin_amdgcn_rcpf(xg_sum(l2[q][0] + l2[q][1]));
#pragma unroll
      for (int s = 0; s < 4; ++s)
        *(LAS bf16x8*)(uintptr_t)(ldsb + OREG + (((4 * jj + s - ksrot) & 7) * 6 + 3 * qh + q) * 1024 + lane * 16) = pack2(acc[2 * s][q] * inv, acc[2 * s + 1][q] * inv);
    }
  }

  const __amdgpu_buffer_rsrc_t rg = mkrsrc(ows + (size_t)grp * 8 * PART, 8 * PART);
  auto await_pass = [&](unsigned pi, unsigned mine) {
#pragma clang loop unroll(disable)
    for (unsigned spin = 0; spin < (1u << 22); ++spin) {
      const unsigned ec = __builtin_amdgcn_raw_buffer_load_b32(rf, 128 * (1 + mem) + 4 * (lane & 7), 32 * pi, 16);
      MEMFENCE();
      if (__all(ec == mine)) break;
      __builtin_amdgcn_s_sleep(1);
    }
  };
  auto sum_pass = [&](auto NS_, unsigned NL, unsigned pi, unsigned r1) {
    constexpr int NS = NS_.value;
    const unsigned p = (pi + prot + 2) % 3;
    if (lane < NL) {
      const unsigned r0 = mem * 288 + r1 + lane;
      u32x4 v[NS][8];
#pragma unroll
      for (int s = 0; s < NS; ++s)
#pragma unroll
        for (int i = 0; i < 8; ++i) v[s][i] = __builtin_amdgcn_raw_buffer_load_b128(rg, (r0 + NL * s) * 16, i * PART + p * 36864, 16);
#pragma unroll
      for (int s = 0; s < NS; ++s) {
        float a[8] = {0.f, 0.f, 0.f, 0.f, 0.f, 0.f, 0.f, 0.f};
#pragma unroll
        for (int i = 0; i < 8; ++i)
#pragma unroll
          for (int k = 0; k < 4; ++k) { a[2 * k] += __uint_as_float(v[s][i][k] << 16); a[2 * k + 1] += __uint_as_float(v[s][i][k] & 0xffff0000u); }
        const unsigned r = r0 + NL * s, ln = r & 63, f = r >> 6, uu = f / 9, i9 = f - 9 * uu, np = i9 / 3, q = i9 - 3 * np;
        const unsigned row = 48 * (uu & 1) + 16 * q + (ln & 15), col = 192 * p + 96 * (uu >> 1) + 32 * np + 4 * (ln >> 4);
        char* op = out + ((size_t)(b * SQ + row) * (2 * DM) + blk * DM + col) * 2;
        *(u32x2*)op = u32x2{cvtpk(a[0], a[1]), cvtpk(a[2], a[3])};
        *(u32x2*)(op + 32) = u32x2{cvtpk(a[4], a[5]), cvtpk(a[6], a[7])};
      }
    }
  };

  if (team == 0) {
    unsigned wof[6];
#pragma unroll
    for (int n = 0; n < 6; ++n) wof[n] = ldsb + (4 * g + q4) * 384 + 16 * ((12 * jj + 2 * n + (p4 >> 1)) ^ ((4 * g + q4) & 6)) + 8 * (p4 & 1);
    const unsigned oof = ldsb + OREG + 3 * qh * 1024 + lane * 16;
    f32x4 oc[6][3];
    unsigned pA[36], pB[36];
#pragma unroll
    for (int i = 0; i < 36; ++i) { pA[i] = 0; pB[i] = 0; }
    unsigned T = 17;
#pragma clang loop unroll(disable)
    for (int p = 0; p < 3; ++p) {
#pragma unroll
      for (int n = 0; n < 6; ++n)
#pragma unroll
        for (int q = 0; q < 3; ++q) oc[n][q] = f32x4{};
#pragma clang loop unroll(disable)
      for (unsigned k = 0; k < 8; k += 2, ++T) {
        WAITV(11); BARRIER();
        bf16x8 of[2][3], wf[2][6];
        const unsigned wb = WREG + (k & 2) * WOU, ob = oof + k * 6144;
#pragma unroll
        for (int i = 0; i < 2; ++i) {
#pragma unroll
          for (int q = 0; q < 3; ++q) of[i][q] = lds_b128(ob + 6144 * i + 1024 * q);
#pragma unroll
          for (int n = 0; n < 6; ++n) wf[i][n] = lds_tr2(wb + wof[n] + WOU * i, wb + wof[n] + WOU * i + 16 * 384);
        }
        wo_issue(2 * T - 31, ldsb + WREG + ((k & 2) ^ 3) * WOU);
        v_issue(T + 1);
        WAITV(14); BARRIER();
        wo_issue(2 * T - 30, ldsb + wb);
        k_issue(T + 2);
#pragma unroll
        for (int n = 0; n < 6; ++n)
#pragma unroll
          for (int q = 0; q < 3; ++q) { oc[n][q] = MFMA16(wf[0][n], of[0][q], oc[n][q]); oc[n][q] = MFMA16(wf[1][n], of[1][q], oc[n][q]); SB0(); }
      }
      if (p == 0) {
#pragma unroll
        for (int i = 0; i < 36; ++i) { const f32x4 v = oc[i / 6][(i / 2) % 3]; pA[i] = cvtpk(v[2 * (i & 1)], v[2 * (i & 1) + 1]); }
      } else if (p == 1) {
#pragma unroll
        for (int i = 0; i < 36; ++i) { const f32x4 v = oc[i / 6][(i / 2) % 3]; pB[i] = cvtpk(v[2 * (i & 1)], v[2 * (i & 1) + 1]); }
      }
    }
    WAITV(14); BARRIER(); wo_issue(27, ldsb + WREG + 3 * WOU); v_issue(30);
    WAITV(14); BARRIER(); k_issue(31);
    WAITV(11); BARRIER(); v_issue(31);
    auto unpark = [&](const unsigned (&p)[36]) {
#pragma unroll
      for (int i = 0; i < 18; ++i) {
        unsigned v0 = p[2 * i], v1 = p[2 * i + 1]; asm volatile("" : "+v"(v0), "+v"(v1));
        oc[i / 3][i % 3] = f32x4{__uint_as_float(v0 << 16), __uint_as_float(v0 & 0xffff0000u), __uint_as_float(v1 << 16), __uint_as_float(v1 & 0xffff0000u)};
      }
    };
    WAITV(8); BARRIER();
    unsigned woh[6];
#pragma unroll
    for (int n = 0; n < 6; ++n) { woh[n] = wof[n] + WREG; asm volatile("" : "+v"(woh[n])); }
    bf16x8 wa[6], wb[6], oa[6], ob[6];
    WAITV(4); BARRIER();
    WAITV(0); BARRIER();
    wo_issue(28, ldsb); wo_issue(29, ldsb + WOU);
    BARRIER();
    wo_issue(30, ldsb + 2 * WOU); wo_issue(31, ldsb + 3 * WOU);
    const __amdgpu_buffer_rsrc_t rp = mkrsrc(ows + (size_t)(grp * 8 + mem) * PART, PART);
    unsigned tick = __builtin_amdgcn_raw_buffer_load_b32(rf, 4 * (lane < 2 ? 2 * u + lane : mem), 0, 16);
    auto publish = [&](unsigned pi) { __builtin_amdgcn_raw_buffer_store_b32(tick, rf, lane < 2 ? 128 * (1 + 2 * u + lane) + 4 * mem : FLG, 32 * pi, 16); };
    auto store_pass = [&](unsigned pi) {
      const unsigned p = (pi + prot + 2) % 3;
#pragma unroll
      for (int n = 0; n < 3; ++n)
#pragma unroll
        for (int q = 0; q < 3; ++q) __builtin_amdgcn_raw_buffer_store_b128(__builtin_bit_cast(u32x4, pack2(oc[2 * n][q], oc[2 * n + 1][q])), rp, lane * 16, p * 36864 + u * 9216 + (n * 3 + q) * 1024, 16);
    };
    auto rd_w2 = [&](const int j, const int hf, const int i) { const int k = 2 * j + (i & 1), n = 3 * hf + (i >> 1); const unsigned a = (k < 4 ? woh[n] : wof[n]) + (k & 3) * WOU; return lds_tr2(a, a + 16 * 384); };
    auto rd_o2 = [&](const int j, const int i) { return lds_b128(oof + (2 * j + i / 3) * 6144 + 1024 * (i % 3)); };
    auto mm18 = [&](const int hf, const bf16x8 (&w)[6], const bf16x8 (&o)[6], auto&& fill) {
#pragma unroll
      for (int m = 0; m < 9; ++m) {
        const int i = m / 3, q = m % 3;
        oc[3 * hf + i][q] = MFMA16(w[2 * i], o[q], oc[3 * hf + i][q]); oc[3 * hf + i][q] = MFMA16(w[2 * i + 1], o[3 + q], oc[3 * hf + i][q]);
        fill(m);
        SB0();
      }
    };
    BARRIER();
#pragma unroll
    for (int i = 0; i < 6; ++i) { oa[i] = rd_o2(0, i); wa[i] = rd_w2(0, 0, i); }
#pragma clang loop unroll(disable)
    for (unsigned p = 0; p < 3; ++p) {
#pragma unroll
      for (int j = 0; j < 4; ++j) {
        bf16x8 (&oc_)[6] = (j & 1) ? ob : oa; bf16x8 (&on_)[6] = (j & 1) ? oa : ob;
        mm18(0, wa, oc_, [&](int m) { if (m < 6) wb[m] = rd_w2(j, 1, m); });
        if (j == 1) { WAITV(0); publish(p > 0 ? p - 1 : 64); }
        __builtin_amdgcn_s_waitcnt(0xC07F);
        if (j < 2) asm volatile("s_barrier" ::: "memory");
        else asm volatile("s_cmp_eq_u32 %0, 2\n\ts_cbranch_scc1 1f\n\ts_barrier\n1:" :: "s"(p) : "scc", "memory");
        SB0();
        mm18(1, wb, oc_, [&](int m) { if (m < 3) { on_[m] = rd_o2((j + 1) & 3, m); on_[m + 3] = rd_o2((j + 1) & 3, m + 3); } else wa[m - 3] = rd_w2((j + 1) & 3, 0, m - 3); });
        MEMFENCE();
      }
      store_pass(p);
      if (p == 0) unpark(pA); else unpark(pB);
    }
    WAITV(0);
    publish(2);
    await_pass(2, __builtin_amdgcn_readlane(tick, 2));
    const unsigned now = __builtin_amdgcn_raw_buffer_load_b32(rf, 4 * (lane < 2 ? 2 * u + lane : mem), 0, 16);
    sum_pass(std::integral_constant<int, 2>{}, 36, 2, 72 * u);
    if (now != tick) { tick = now; publish(0); publish(1); publish(2); }
  } else {
    const unsigned mine = __builtin_amdgcn_raw_buffer_load_b32(rf, 4 * mem, 0, 16);
#pragma clang loop unroll(disable)
    for (unsigned j = 0; j < 11; ++j) {
      if (j < 10) WAITV(12); else WAITV(0);
      BARRIER();
      if (j >= 1 && j < 9) {
        const unsigned k = 2 * j + 6, buf = ldsb + ((k & 4) ? 0 : WREG) + (k & 3) * WOU;
        wo_issue(24 + k, buf); wo_issue(25 + k, buf + WOU);
      }
    }
    await_pass(jj, mine);
    sum_pass(std::integral_constant<int, 3>{}, 48, jj, 144 * qh);
  }
  asm volatile("s_waitcnt vmcnt(0)\nattn_code_end:" ::: "memory");
}

extern "C" void kernel_launch(void* const* d_in, const int* in_sizes, int n_in, void* d_out, int out_size, void* d_ws, size_t ws_size,
                              hipStream_t stream) {
  (void)in_sizes; (void)n_in; (void)out_size; (void)ws_size;
  (void)hipFuncSetAttribute((const void*)attn_main, hipFuncAttributeMaxDynamicSharedMemorySize, LDS_MAIN);
  attn_main<<<256, 512, LDS_MAIN, stream>>>((const char*)d_in[0], (const char*)d_in[1], (const char*)d_in[2], (const char*)d_in[3], (const char*)d_in[4],
                                            (const char*)d_in[5], (const char*)d_in[6], (const char*)d_in[7], (const char*)d_in[8], (const char*)d_in[9], (char*)d_ws, (char*)d_out);
}
```

```cpp
#include <hip/hip_runtime.h>
#include <hip/hip_bf16.h>
#include <stdint.h>
#include <type_traits>

#define LAS __attribute__((address_space(3)))
#define DI __device__ __forceinline__
typedef __bf16 bf16x2 __attribute__((ext_vector_type(2)));
typedef __bf16 bf16x4 __attribute__((ext_vector_type(4)));
typedef __bf16 bf16x8 __attribute__((ext_vector_type(8)));
typedef float f32x2 __attribute__((ext_vector_type(2)));
typedef float f32x4 __attribute__((ext_vector_type(4)));
typedef unsigned u32x4 __attribute__((ext_vector_type(4)));
typedef unsigned u32x2 __attribute__((ext_vector_type(2)));

constexpr int NB = 16, SQ = 96, DM = 576, NH = 32, SK = 512, DH = 128, HD = NH * DH;
constexpr unsigned LDS_MAIN = 163840;
constexpr float SC_LOG2E = 0.08838834764831845f * 1.4426950408889634f;
constexpr float THR = 8.0f;

DI unsigned rfl(unsigned x) { return __builtin_amdgcn_readfirstlane(x); }
DI unsigned cvtpk(float lo, float hi) { f32x2 v = {lo, hi}; bf16x2 b = __builtin_convertvector(v, bf16x2); return __builtin_bit_cast(unsigned, b); }
DI bf16x8 pack2(const f32x4& a, const f32x4& b) {
  u32x4 w = {cvtpk(a[0], a[1]), cvtpk(a[2], a[3]), cvtpk(b[0], b[1]), cvtpk(b[2], b[3])};
  return __builtin_bit_cast(bf16x8, w);
}
DI bf16x8 pack_swap(const f32x4& a0, const f32x4& a1) {
  unsigned p00 = cvtpk(a0[0], a0[1]), p01 = cvtpk(a0[2], a0[3]), p10 = cvtpk(a1[0], a1[1]), p11 = cvtpk(a1[2], a1[3]);
  auto r0 = __builtin_amdgcn_permlane32_swap(p00, p10, false, false);
  auto r1 = __builtin_amdgcn_permlane32_swap(p01, p11, false, false);
  auto t0 = __builtin_amdgcn_permlane16_swap(r0[0], r0[1], false, false);
  auto t1 = __builtin_amdgcn_permlane16_swap(r1[0], r1[1], false, false);
  u32x4 w = {t0[0], t1[0], t0[1], t1[1]};
  return __builtin_bit_cast(bf16x8, w);
}
DI bf16x8 lds_b128(unsigned a) { return *(const LAS bf16x8*)(uintptr_t)a; }
DI bf16x8 lds_tr2(unsigned a0, unsigned a1) {
  bf16x4 lo = __builtin_amdgcn_ds_read_tr16_b64_v4bf16((LAS bf16x4*)(uintptr_t)a0);
  bf16x4 hi = __builtin_amdgcn_ds_read_tr16_b64_v4bf16((LAS bf16x4*)(uintptr_t)a1);
  return __builtin_shufflevector(lo, hi, 0, 1, 2, 3, 4, 5, 6, 7);
}
DI bf16x4 lds_tr1(unsigned a) { return __builtin_amdgcn_ds_read_tr16_b64_v4bf16((LAS bf16x4*)(uintptr_t)a); }
DI void set_half(bf16x8& f, const int hi, bf16x4 v) { f = hi ? __builtin_shufflevector(f, __builtin_shufflevector(v, v, 0, 1, 2, 3, 0, 1, 2, 3), 0, 1, 2, 3, 12, 13, 14, 15) : __builtin_shufflevector(v, v, 0, 1, 2, 3, -1, -1, -1, -1); }
DI float max2f(float a, float b) { return __builtin_elementwise_maximum(a, b); }
DI float max3f(float a, float b, float c) { return max2f(max2f(a, b), c); }
DI float xg_max(float v) {
  auto r = __builtin_amdgcn_permlane32_swap(__float_as_uint(v), __float_as_uint(v), false, false); v = max2f(__uint_as_float(r[0]), __uint_as_float(r[1]));
  auto s = __builtin_amdgcn_permlane16_swap(__float_as_uint(v), __float_as_uint(v), false, false); return max2f(__uint_as_float(s[0]), __uint_as_float(s[1]));
}
DI float xg_sum(float v) {
  auto r = __builtin_amdgcn_permlane32_swap(__float_as_uint(v), __float_as_uint(v), false, false); v = __uint_as_float(r[0]) + __uint_as_float(r[1]);
  auto s = __builtin_amdgcn_permlane16_swap(__float_as_uint(v), __float_as_uint(v), false, false); return __uint_as_float(s[0]) + __uint_as_float(s[1]);
}
#define MFMA16(a, b, c) __builtin_amdgcn_mfma_f32_16x16x32_bf16((a), (b), (c), 0, 0, 0)
#define SB0() __builtin_amdgcn_sched_barrier(0)
#define MEMFENCE() asm volatile("" ::: "memory")
#define BARRIER() do { SB0(); __builtin_amdgcn_s_waitcnt(0xC07F); asm volatile("s_barrier" ::: "memory"); SB0(); } while (0)
#define WAITV(n) asm volatile("s_waitcnt vmcnt(" #n ")" ::: "memory")
DI __amdgpu_buffer_rsrc_t mkrsrc(const void* p, unsigned bytes) { return __builtin_amdgcn_make_buffer_rsrc((void*)p, (short)0, (int)bytes, 0x00020000); }
DI void dma4_nt(__amdgpu_buffer_rsrc_t r, unsigned lds, unsigned va, unsigned vb, unsigned so) {
  asm volatile("s_mov_b32 m0, %0\n\ts_nop 0\n\t"
               "buffer_load_dwordx4 %1, %3, %4 offen nt lds\n\t"
               "buffer_load_dwordx4 %1, %3, %4 offen offset:1024 nt lds\n\t"
               "buffer_load_dwordx4 %2, %3, %4 offen offset:2048 nt lds\n\t"
               "buffer_load_dwordx4 %2, %3, %4 offen offset:3072 nt lds"
               :: "s"(lds), "v"(va), "v"(vb), "s"(r), "s"(so) : "memory");
}
DI void dma4(__amdgpu_buffer_rsrc_t r, unsigned lds, unsigned v0, unsigned v1, unsigned v2, unsigned v3, unsigned so) {
  asm volatile("s_mov_b32 m0, %0\n\ts_nop 0\n\t"
               "buffer_load_dwordx4 %1, %5, %6 offen lds\n\t"
               "buffer_load_dwordx4 %2, %5, %6 offen offset:1024 lds\n\t"
               "buffer_load_dwordx4 %3, %5, %6 offen offset:2048 lds\n\t"
               "buffer_load_dwordx4 %4, %5, %6 offen offset:3072 lds"
               :: "s"(lds), "v"(v0), "v"(v1), "v"(v2), "v"(v3), "s"(r), "s"(so) : "memory");
}
DI void dma3(__amdgpu_buffer_rsrc_t r, unsigned lds, unsigned v0, unsigned v1, unsigned v2, unsigned so) {
  asm volatile("s_mov_b32 m0, %0\n\ts_nop 0\n\t"
               "buffer_load_dwordx4 %1, %4, %5 offen lds\n\t"
               "buffer_load_dwordx4 %2, %4, %5 offen offset:1024 lds\n\t"
               "buffer_load_dwordx4 %3, %4, %5 offen offset:2048 lds"
               :: "s"(lds), "v"(v0), "v"(v1), "v"(v2), "s"(r), "s"(so) : "memory");
}
DI void dma3w(__amdgpu_buffer_rsrc_t r, unsigned lds, unsigned v0, unsigned v1, unsigned v2, unsigned so) {
  asm volatile("s_mov_b32 m0, %0\n\ts_nop 3\n\t"
               "buffer_load_dwordx4 %1, %4, %5 offen lds\n\t"
               "buffer_load_dwordx4 %2, %4, %5 offen offset:1024 lds\n\t"
               "buffer_load_dwordx4 %3, %4, %5 offen offset:2048 lds"
               :: "s"(lds), "v"(v0), "v"(v1), "v"(v2), "s"(r), "s"(so) : "memory");
}
DI void touch(__amdgpu_buffer_rsrc_t r, unsigned lds, unsigned v, unsigned so) {
  asm volatile("s_mov_b32 m0, %0\n\ts_nop 0\n\tbuffer_load_dword %1, %2, %3 offen lds" :: "s"(lds), "v"(v), "s"(r), "s"(so) : "memory");
}

constexpr unsigned KVS = 16384;
constexpr unsigned QB1 = 73728, QB2 = 118784;
constexpr unsigned SCR = 65536;
constexpr unsigned OREG = 65536, WREG = 114688, WOU = 12288;
constexpr unsigned FLG = 2048;
constexpr unsigned PART = 110592;

__global__ __launch_bounds__(512, 2) void attn_main(const char* xA, const char* kA, const char* vA, const char* wqA, const char* woA,
                                                    const char* xB, const char* kB, const char* vB, const char* wqB, const char* woB, char* ows, char* out) {
  extern __shared__ __attribute__((aligned(16))) char lds[];
  const unsigned ldsb = rfl((unsigned)(uintptr_t)(LAS char*)lds);
  const unsigned wid = rfl(threadIdx.x >> 6), lane = threadIdx.x & 63, team = wid >> 2, u = wid & 3, jj = u >> 1, qh = u & 1;
  const unsigned id = blockIdx.x, xcd = id & 7, idx = id >> 3, blk = xcd >> 2;
  const unsigned b = idx & 15, hb = 8 * (xcd & 3) + 4 * (idx >> 4), h = hb + 2 * team + jj;
  const char* kp = blk ? kB : kA; const char* vp = blk ? vB : vA; const char* wop = blk ? woB : woA;
  const __amdgpu_buffer_rsrc_t rk = mkrsrc(kp + (size_t)(b * NH + hb) * SK * DH * 2, 4 * SK * DH * 2);
  const __amdgpu_buffer_rsrc_t rv = mkrsrc(vp + (size_t)(b * NH + hb) * SK * DH * 2, 4 * SK * DH * 2);
  const __amdgpu_buffer_rsrc_t ro = mkrsrc(wop, HD * DM * 2);
  const unsigned grp = blk * NB + b, mem = hb >> 2;
  const __amdgpu_buffer_rsrc_t rf = mkrsrc(ows + (size_t)256 * PART + grp * FLG, FLG);
  const unsigned g = lane >> 4, c16 = lane & 15, q4 = c16 >> 2, p4 = lane & 3;
  const unsigned vka = g * 256 + 16 * (c16 ^ ((8 * qh) | g)), vkb = g * 256 + 16 * (c16 ^ ((8 * qh + 4) | g));
  const unsigned vva = g * 256 + 16 * (c16 ^ (g << 2)), vvb = g * 256 + 16 * (c16 ^ ((g << 2) | 2));
  const unsigned krot = (id * 5 + (id >> 3) + jj * 7) & 15;
  const unsigned kvs0 = jj * (SK * DH * 2) + 4096 * qh, kvd0 = ldsb + 8192 * jj + 4096 * qh;
  auto kv_so = [&](unsigned T) { return kvs0 + ((T & 16) << 14) + (((T + krot) & 15) << 13); };
  auto k_issue = [&](unsigned T) { dma4_nt(rk, kvd0 + 3 * KVS - (T & 1) * (2 * KVS), vka, vkb, kv_so(T)); };
  auto v_issue = [&](unsigned T) { dma4_nt(rv, kvd0 + (T & 1) * (2 * KVS), vva, vvb, kv_so(T)); };
  const unsigned tro = 256 * (8 * g + q4) + 16 * ((q4 << 2) | ((g & 1) << 1) | (p4 >> 1)) + 8 * (p4 & 1);
  const unsigned xo0 = 32768 + (48 * qh + c16) * 128 + 16 * (g ^ ((c16 >> 1) & 7));
  auto rd_tr8 = [&](bf16x8 (&f)[8], unsigned a, unsigned imm) {
#pragma unroll
    for (int d = 0; d < 8; ++d) f[d] = lds_tr2((a ^ (32 * d)) + imm, (a ^ (32 * d)) + imm + 1024);
  };

  bf16x8 qf[4][3];
  f32x4 acc[8][3];
  auto zero_acc = [&]() {
#pragma unroll
    for (int d = 0; d < 8; ++d)
#pragma unroll
      for (int q = 0; q < 3; ++q) acc[d][q] = f32x4{};
  };
  auto mm24 = [&](const int hf, const bf16x8 (&w)[8], bf16x8 (&x)[6], auto&& fill) {
#pragma unroll
    for (int m = 0; m < 24; ++m) {
      const int q = m >> 3, i = (m >> 1) & 3, ks = m & 1;
      acc[4 * hf + i][q] = MFMA16(w[2 * i + ks], x[3 * ks + q], acc[4 * hf + i][q]);
      fill(m);
      SB0();
    }
  };
  auto nofill = [](int) {};
  auto rd_wh = [&](bf16x8 (&w)[8], const int hf, const int r, unsigned a) { const int i = r >> 1; set_half(w[i], r & 1, lds_tr1((a ^ (128 * hf + 32 * (i >> 1))) + 8192 * (i & 1) + 1024 * (r & 1))); };
  auto rd_x1 = [&](const int i, unsigned xo) { return lds_b128((xo ^ (64 * (i / 3))) + 2048 * (i % 3)); };
  auto rd_w = [&](const int hf, bf16x8 (&w)[8], unsigned a) {
#pragma unroll
    for (int i = 0; i < 8; ++i) { const unsigned p = (a ^ (128 * hf + 32 * (i >> 1))) + 8192 * (i & 1); w[i] = lds_tr2(p, p + 1024); }
  };
  auto rd_x = [&](bf16x8 (&x)[6], unsigned xo) {
#pragma unroll
    for (int i = 0; i < 6; ++i) x[i] = lds_b128((xo ^ (64 * (i / 3))) + 2048 * (i % 3));
  };
  auto pack_q = [&]() {
#pragma unroll
    for (int s = 0; s < 4; ++s)
#pragma unroll
      for (int q = 0; q < 3; ++q) qf[s][q] = pack_swap(acc[2 * s][q] * SC_LOG2E, acc[2 * s + 1][q] * SC_LOG2E);
  };

  unsigned long long pc0; unsigned clen;
  asm volatile("attn_code_begin:\n\ts_getpc_b64 %0\n\ts_mov_b32 %1, attn_code_end-attn_code_begin-8" : "=s"(pc0), "=s"(clen));
  if (team == 0) {
    if (u == 1) touch(mkrsrc((const void*)pc0, clen), ldsb + SCR + 256, (idx * 8 + (lane & 7)) * 128, 0);
    if (u == 0) {
      const unsigned old = __builtin_amdgcn_raw_buffer_load_b32(rf, lane < 24 ? 128 * (1 + mem) + 4 * lane : 4 * mem, 0, 16);
      const unsigned base = __builtin_amdgcn_readlane(old, 24) + 1;
      unsigned k = 0;
      while (__any(old - base == k)) ++k;
      if (lane == 0) __builtin_amdgcn_raw_buffer_store_b32(base + k, rf, 4 * mem, 0, 16);
    }
    zero_acc();
    bf16x8 wa[8], wb[8], xa[6];
#pragma unroll
    for (int d = 0; d < 8; ++d) wb[d] = bf16x8{};
#pragma unroll
    for (int q = 0; q < 6; ++q) xa[q] = bf16x8{};
    unsigned bufb = ldsb;
#pragma clang loop unroll(disable)
    for (int c = 0; c < 9; ++c) {
      const unsigned a = bufb + 16384 * jj + tro, xo = bufb + xo0;
      bufb = bufb == ldsb ? ldsb + QB1 : bufb == ldsb + QB1 ? ldsb + QB2 : ldsb;
      BARRIER();
      mm24(1, wb, xa, [&](int m) {
        if (m == 12 || m == 13) xa[3 * (m & 1)] = rd_x1(3 * (m & 1), xo);
        else if (m == 20 || m == 21) xa[3 * (m & 1) + 1] = rd_x1(3 * (m & 1) + 1, xo);
        else if (m < 18) rd_wh(wa, 0, m < 12 ? m : m - 2, a); });
      mm24(0, wa, xa, [&](int m) { if (m == 4 || m == 5) xa[3 * (m & 1) + 2] = rd_x1(3 * (m & 1) + 2, xo); else if (m < 18) rd_wh(wb, 1, m < 4 ? m : m - 2, a); });
    }
    mm24(1, wb, xa, nofill);
    pack_q();
  } else {
    const char* xp = blk ? xB : xA; const char* wp = blk ? wqB : wqA;
    const __amdgpu_buffer_rsrc_t rx = mkrsrc(xp + (size_t)b * SQ * DM * 2, SQ * DM * 2);
    const __amdgpu_buffer_rsrc_t rw = mkrsrc(wp, DM * HD * 2);
    unsigned vw[4], vx[3];
#pragma unroll
    for (int pp = 0; pp < 4; ++pp) vw[pp] = (4 * pp + g) * (HD * 2) - 1024 * pp + 16 * (c16 ^ ((g << 2) | (pp & 2)));
#pragma unroll
    for (int i = 0; i < 3; ++i) vx[i] = (8 * i + (lane >> 3)) * (DM * 2) - 1024 * i + 16 * ((lane & 7) ^ ((4 * (3 * u + i) + g) & 7));
    const unsigned qrot = (b + 4 * (idx >> 4)) % 9;
    auto x_issue = [&](unsigned c, unsigned buf) { dma3(rx, buf + 32768 + 3072 * u, vx[0], vx[1], vx[2], c * 128 + 24 * u * (DM * 2)); };
    auto w_issue = [&](unsigned c, unsigned ks, unsigned buf, unsigned head) {
      dma4(rw, buf + 16384 * jj + 8192 * ks + 4096 * qh, vw[0], vw[1], vw[2], vw[3], (c * 64 + 32 * ks + 16 * qh) * (HD * 2) + head * 256);
    };
    auto nxt = [&](unsigned c) { return c == 8 ? 0u : c + 1; };
    unsigned cq = qrot;
    x_issue(cq, ldsb); w_issue(cq, 0, ldsb, h - 2); w_issue(cq, 1, ldsb, h - 2); cq = nxt(cq);
    x_issue(cq, ldsb + QB1); w_issue(cq, 0, ldsb + QB1, h - 2); w_issue(cq, 1, ldsb + QB1, h - 2); cq = nxt(cq);
    { unsigned c = 2 + (lane >= 24) + (lane >= 48) + qrot; if (c >= 9) c -= 9;
      touch(rx, ldsb + SCR, (24 * u + lane % 24) * (DM * 2) + c * 128, 0);
      c = 4 + qrot; if (c >= 9) c -= 9;
      touch(rx, ldsb + SCR, (24 * u + 16 + (lane & 7)) * (DM * 2) + c * 128, 0); }
    unsigned bufb = ldsb + QB2;
    WAITV(13);
#pragma clang loop unroll(disable)
    for (int c = 0; c < 7; ++c) {
      BARRIER();
      x_issue(cq, bufb); w_issue(cq, 0, bufb, h - 2); w_issue(cq, 1, bufb, h - 2); cq = nxt(cq);
      bufb = bufb == ldsb ? ldsb + QB1 : bufb == ldsb + QB1 ? ldsb + QB2 : ldsb;
      WAITV(11);
    }
    k_issue(0);
    BARRIER();
    v_issue(0);
    WAITV(8); BARRIER();
    k_issue(1); v_issue(1);
    zero_acc();
    WAITV(12); BARRIER();
    x_issue(cq, ldsb + QB1); w_issue(cq, 0, ldsb + QB1, h); w_issue(cq, 1, ldsb + QB1, h); cq = nxt(cq);
    WAITV(19); BARRIER();
    k_issue(2);
    unsigned T = 1;
#pragma clang loop unroll(disable)
    for (int c = 0; c < 9; ++c) {
      const unsigned qb = ldsb + ((c & 1) ? QB2 : QB1), qn = ldsb + ((c & 1) ? QB1 : QB2);
      const bool fast = c & 1;
      bf16x8 wf[2][8], xf[6];
      if (c == 0) WAITV(4); else WAITV(8);
      BARRIER();
      rd_x(xf, qb + xo0);
      rd_w(0, wf[0], qb + 16384 * jj + tro);
      if (!fast) {
        if (c < 8) { x_issue(cq, qn); w_issue(cq, 0, qn, h); }
        v_issue(T + 1);
        if (c < 8) WAITV(15); else WAITV(8);
        BARRIER();
        k_issue(T + 2); ++T;
        mm24(0, wf[0], xf, nofill);
        if (c < 8) WAITV(15); else WAITV(8);
        BARRIER();
      } else { x_issue(cq, qn); w_issue(cq, 0, qn, h); }
      rd_w(1, wf[1], qb + 16384 * jj + tro);
      if (c < 8) w_issue(cq, 1, qn, h);
      cq = nxt(cq);
      v_issue(T + 1);
      if (fast) WAITV(19); else if (c < 8) WAITV(12); else WAITV(8);
      BARRIER();
      k_issue(T + 2); ++T;
      if (fast) mm24(0, wf[0], xf, nofill);
      mm24(1, wf[1], xf, nofill);
    }
    WAITV(8); BARRIER();
    v_issue(16);
    pack_q();
    WAITV(8); BARRIER();
    k_issue(17);
  }

  const unsigned prot = (b >> 3) + (b & 1), ksrot = b & 7;
  unsigned vo[3];
#pragma unroll
  for (int i = 0; i < 3; ++i) { const unsigned L = 64 * i + lane, row = L / 24, ph = L - 24 * row; vo[i] = row * (DM * 2) + 16 * (ph ^ (row & 6)) - 1024 * i; }
  unsigned wo_tab;
  { const unsigned r = lane >= 24, k = lane - 24 * r; unsigned p = ((k >> 3) + prot + 2 * r) % 3;
    wo_tab = ((hb + 2 * r) * DH + 32 * ((k + ksrot) & 7) + 8 * u) * (DM * 2) + 384 * p; }
  auto wo_issue = [&](unsigned i, unsigned buf) { dma3w(ro, buf + 3072 * u, vo[0], vo[1], vo[2], __builtin_amdgcn_readlane(wo_tab, i)); };

  {
    zero_acc();
    f32x4 negm[3] = {f32x4{}, f32x4{}, f32x4{}};
    f32x2 l2[3] = {f32x2{}, f32x2{}, f32x2{}};
    const unsigned koff = ldsb + KVS + 8192 * jj + 256 * (8 * q4 + p4) + 16 * (g ^ c16), voff = ldsb + 8192 * jj + tro;
    f32x4 sc[2][3];
    bf16x8 kf[2][4], vf[8], pf[3];
    auto qk = [&]() {
#pragma unroll
      for (int kb = 0; kb < 2; ++kb)
#pragma unroll
        for (int q = 0; q < 3; ++q) {
          sc[kb][q] = MFMA16(kf[kb][0], qf[0][q], negm[q]);
#pragma unroll
          for (int s = 1; s < 4; ++s) sc[kb][q] = MFMA16(kf[kb][s], qf[s][q], sc[kb][q]);
          SB0();
        }
    };
    WAITV(4);
    BARRIER();
#pragma unroll
    for (int s = 0; s < 4; ++s) { const unsigned a = (2 * KVS + koff) ^ (64 * s); kf[0][s] = lds_b128(a); kf[1][s] = lds_b128(a + 1024); }
    qk();
#pragma clang loop unroll(disable)
    for (int t = 0; t < 16; ++t) {
      const unsigned sb = (t & 1) * (2 * KVS);
      BARRIER();
      rd_tr8(vf, sb + voff, 0);
      MEMFENCE(); SB0();
      float mx[3];
#pragma unroll
      for (int q = 0; q < 3; ++q)
        mx[q] = max3f(max3f(sc[0][q][0], sc[0][q][1], sc[0][q][2]), max3f(sc[0][q][3], sc[1][q][0], sc[1][q][1]), max2f(sc[1][q][2], sc[1][q][3]));
      if (t == 0 || __builtin_expect(__any(!(max3f(mx[0], mx[1], mx[2]) <= THR)), 0)) {
#pragma unroll
        for (int q = 0; q < 3; ++q) {
          float dl = xg_max(mx[q]);
          if (t != 0) dl = max2f(dl, 0.f);
          negm[q] -= dl; sc[0][q] -= dl; sc[1][q] -= dl; mx[q] = dl;
        }
        if (t != 0) {
#pragma unroll
          for (int q = 0; q < 3; ++q) {
            const float al = __builtin_amdgcn_exp2f(-mx[q]);
            l2[q] *= al;
#pragma unroll
            for (int d = 0; d < 8; ++d) acc[d][q] *= al;
          }
        }
      }
#pragma unroll
      for (int q = 0; q < 3; ++q) {
#pragma unroll
        for (int i = 0; i < 4; ++i) { sc[0][q][i] = __builtin_amdgcn_exp2f(sc[0][q][i]); sc[1][q][i] = __builtin_amdgcn_exp2f(sc[1][q][i]); }
        { const f32x4 s4 = sc[0][q] + sc[1][q]; l2[q] += f32x2{s4[0], s4[1]} + f32x2{s4[2], s4[3]}; }
        pf[q] = pack2(sc[0][q], sc[1][q]);
      }
      asm volatile("" : "+v"(pf[0]), "+v"(pf[1]), "+v"(pf[2]), "+v"(l2[0]), "+v"(l2[1]), "+v"(l2[2]));
      WAITV(0); BARRIER();
      if (t == 15 && team == 0) { wo_issue(0, ldsb + WREG); wo_issue(1, ldsb + WREG + WOU); v_issue(17); }
#pragma unroll
      for (int d = 0; d < 8; ++d) {
#pragma unroll
        for (int q = 0; q < 3; ++q) acc[d][q] = MFMA16(vf[d], pf[q], acc[d][q]);
        if (d >= 2 && d < 6) { const int kb = (d - 2) >> 1, s = (d & 1) * 2; kf[kb][s] = lds_b128(((sb + koff) ^ (64 * s)) + 1024 * kb); kf[kb][s + 1] = lds_b128(((sb + koff) ^ (64 * s + 64)) + 1024 * kb); MEMFENCE(); }
        SB0();
      }
      if (t < 15) qk();
    }
    if (team == 0) { BARRIER(); wo_issue(2, ldsb + WREG + 2 * WOU); k_issue(18); }
#pragma unroll
    for (int q = 0; q < 3; ++q) {
      const float inv = __builtin_amdgcn_rcpf(xg_sum(l2[q][0] + l2[q][1]));
#pragma unroll
      for (int s = 0; s < 4; ++s)
        *(LAS bf16x8*)(uintptr_t)(ldsb + OREG + (((4 * jj + s - ksrot) & 7) * 6 + 3 * qh + q) * 1024 + lane * 16) = pack2(acc[2 * s][q] * inv, acc[2 * s + 1][q] * inv);
    }
  }

  const __amdgpu_buffer_rsrc_t rg = mkrsrc(ows + (size_t)grp * 8 * PART, 8 * PART);
  auto await_pass = [&](unsigned pi, unsigned mine) {
#pragma clang loop unroll(disable)
    for (unsigned spin = 0; spin < (1u << 22); ++spin) {
      const unsigned ec = __builtin_amdgcn_raw_buffer_load_b32(rf, 128 * (1 + mem) + 4 * (lane & 7), 32 * pi, 16);
      MEMFENCE();
      if (__all(ec == mine)) break;
      __builtin_amdgcn_s_sleep(1);
    }
  };
  auto sum_pass = [&](auto NS_, unsigned NL, unsigned pi, unsigned r1) {
    constexpr int NS = NS_.value;
    const unsigned p = (pi + prot + 2) % 3;
    if (lane < NL) {
      const unsigned r0 = mem * 288 + r1 + lane;
      u32x4 v[NS][8];
#pragma unroll
      for (int s = 0; s < NS; ++s)
#pragma unroll
        for (int i = 0; i < 8; ++i) v[s][i] = __builtin_amdgcn_raw_buffer_load_b128(rg, (r0 + NL * s) * 16, i * PART + p * 36864, 16);
#pragma unroll
      for (int s = 0; s < NS; ++s) {
        float a[8] = {0.f, 0.f, 0.f, 0.f, 0.f, 0.f, 0.f, 0.f};
#pragma unroll
        for (int i = 0; i < 8; ++i)
#pragma unroll
          for (int k = 0; k < 4; ++k) { a[2 * k] += __uint_as_float(v[s][i][k] << 16); a[2 * k + 1] += __uint_as_float(v[s][i][k] & 0xffff0000u); }
        const unsigned r = r0 + NL * s, ln = r & 63, f = r >> 6, uu = f / 9, i9 = f - 9 * uu, np = i9 / 3, q = i9 - 3 * np;
        const unsigned row = 48 * (uu & 1) + 16 * q + (ln & 15), col = 192 * p + 96 * (uu >> 1) + 32 * np + 4 * (ln >> 4);
        char* op = out + ((size_t)(b * SQ + row) * (2 * DM) + blk * DM + col) * 2;
        *(u32x2*)op = u32x2{cvtpk(a[0], a[1]), cvtpk(a[2], a[3])};
        *(u32x2*)(op + 32) = u32x2{cvtpk(a[4], a[5]), cvtpk(a[6], a[7])};
      }
    }
  };

  if (team == 0) {
    unsigned wof[6];
#pragma unroll
    for (int n = 0; n < 6; ++n) wof[n] = ldsb + (4 * g + q4) * 384 + 16 * ((12 * jj + 2 * n + (p4 >> 1)) ^ ((4 * g + q4) & 6)) + 8 * (p4 & 1);
    const unsigned oof = ldsb + OREG + 3 * qh * 1024 + lane * 16;
    f32x4 oc[6][3];
    unsigned pA[36], pB[36];
#pragma unroll
    for (int i = 0; i < 36; ++i) { pA[i] = 0; pB[i] = 0; }
    unsigned T = 17;
#pragma clang loop unroll(disable)
    for (int p = 0; p < 3; ++p) {
#pragma unroll
      for (int n = 0; n < 6; ++n)
#pragma unroll
        for (int q = 0; q < 3; ++q) oc[n][q] = f32x4{};
#pragma clang loop unroll(disable)
      for (unsigned k = 0; k < 8; k += 2, ++T) {
        WAITV(11); BARRIER();
        bf16x8 of[2][3], wf[2][6];
        const unsigned wb = WREG + (k & 2) * WOU, ob = oof + k * 6144;
#pragma unroll
        for (int i = 0; i < 2; ++i) {
#pragma unroll
          for (int q = 0; q < 3; ++q) of[i][q] = lds_b128(ob + 6144 * i + 1024 * q);
#pragma unroll
          for (int n = 0; n < 6; ++n) wf[i][n] = lds_tr2(wb + wof[n] + WOU * i, wb + wof[n] + WOU * i + 16 * 384);
        }
        wo_issue(2 * T - 31, ldsb + WREG + ((k & 2) ^ 3) * WOU);
        v_issue(T + 1);
        WAITV(14); BARRIER();
        wo_issue(2 * T - 30, ldsb + wb);
        k_issue(T + 2);
#pragma unroll
        for (int n = 0; n < 6; ++n)
#pragma unroll
          for (int q = 0; q < 3; ++q) { oc[n][q] = MFMA16(wf[0][n], of[0][q], oc[n][q]); oc[n][q] = MFMA16(wf[1][n], of[1][q], oc[n][q]); SB0(); }
      }
      if (p == 0) {
#pragma unroll
        for (int i = 0; i < 36; ++i) { const f32x4 v = oc[i / 6][(i / 2) % 3]; pA[i] = cvtpk(v[2 * (i & 1)], v[2 * (i & 1) + 1]); }
      } else if (p == 1) {
#pragma unroll
        for (int i = 0; i < 36; ++i) { const f32x4 v = oc[i / 6][(i / 2) % 3]; pB[i] = cvtpk(v[2 * (i & 1)], v[2 * (i & 1) + 1]); }
      }
    }
    WAITV(14); BARRIER(); wo_issue(27, ldsb + WREG + 3 * WOU); v_issue(30);
    WAITV(14); BARRIER(); k_issue(31);
    WAITV(11); BARRIER(); v_issue(31);
    auto unpark = [&](const unsigned (&p)[36]) {
#pragma unroll
      for (int i = 0; i < 18; ++i) {
        unsigned v0 = p[2 * i], v1 = p[2 * i + 1]; asm volatile("" : "+v"(v0), "+v"(v1));
        oc[i / 3][i % 3] = f32x4{__uint_as_float(v0 << 16), __uint_as_float(v0 & 0xffff0000u), __uint_as_float(v1 << 16), __uint_as_float(v1 & 0xffff0000u)};
      }
    };
    WAITV(8); BARRIER();
    unsigned woh[6];
#pragma unroll
    for (int n = 0; n < 6; ++n) { woh[n] = wof[n] + WREG; asm volatile("" : "+v"(woh[n])); }
    bf16x8 wa[6], wb[6], oa[6];
    WAITV(4); BARRIER();
    WAITV(0); BARRIER();
    wo_issue(28, ldsb); wo_issue(29, ldsb + WOU);
    BARRIER();
    wo_issue(30, ldsb + 2 * WOU); wo_issue(31, ldsb + 3 * WOU);
    const __amdgpu_buffer_rsrc_t rp = mkrsrc(ows + (size_t)(grp * 8 + mem) * PART, PART);
    unsigned tick = __builtin_amdgcn_raw_buffer_load_b32(rf, 4 * (lane < 2 ? 2 * u + lane : mem), 0, 16);
    auto publish = [&](unsigned pi) { __builtin_amdgcn_raw_buffer_store_b32(tick, rf, lane < 2 ? 128 * (1 + 2 * u + lane) + 4 * mem : FLG, 32 * pi, 16); };
    auto store_pass = [&](unsigned pi) {
      const unsigned p = (pi + prot + 2) % 3;
#pragma unroll
      for (int n = 0; n < 3; ++n)
#pragma unroll
        for (int q = 0; q < 3; ++q) __builtin_amdgcn_raw_buffer_store_b128(__builtin_bit_cast(u32x4, pack2(oc[2 * n][q], oc[2 * n + 1][q])), rp, lane * 16, p * 36864 + u * 9216 + (n * 3 + q) * 1024, 16);
    };
    auto rd_w2 = [&](const int j, const int hf, const int i) { const int k = 2 * j + (i & 1), n = 3 * hf + (i >> 1); const unsigned a = (k < 4 ? woh[n] : wof[n]) + (k & 3) * WOU; return lds_tr2(a, a + 16 * 384); };
    auto rd_o2 = [&](const int j, const int i) { return lds_b128(oof + (2 * j + i / 3) * 6144 + 1024 * (i % 3)); };
    auto rd_w2h = [&](bf16x8 (&w)[6], const int j, const int hf, const int r) {
      const int i = r >> 1, k = 2 * j + (i & 1), n = 3 * hf + (i >> 1);
      set_half(w[i], r & 1, lds_tr1((k < 4 ? woh[n] : wof[n]) + (k & 3) * WOU + 16 * 384 * (r & 1)));
    };
    auto mm18 = [&](const int hf, const bf16x8 (&w)[6], bf16x8 (&o)[6], auto&& fill) {
#pragma unroll
      for (int m = 0; m < 18; ++m) {
        const int q = m / 6, i = (m % 6) >> 1, un = m & 1;
        oc[3 * hf + i][q] = MFMA16(w[2 * i + un], o[3 * un + q], oc[3 * hf + i][q]);
        fill(m);
        SB0();
      }
    };
    BARRIER();
#pragma unroll
    for (int i = 0; i < 6; ++i) { oa[i] = rd_o2(0, i); wa[i] = rd_w2(0, 0, i); }
#pragma clang loop unroll(disable)
    for (unsigned p = 0; p < 3; ++p) {
#pragma unroll
      for (int j = 0; j < 4; ++j) {
        mm18(0, wa, oa, [&](int m) { if (m == 3 || m == 4) oa[3 * (m & 1) + 2] = rd_o2(j, 3 * (m & 1) + 2); else if (m < 14) rd_w2h(wb, j, 1, m < 3 ? m : m - 2); });
        if (j == 1) { WAITV(0); publish(p > 0 ? p - 1 : 64); }
        __builtin_amdgcn_s_waitcnt(0xC07F);
        if (j < 2) asm volatile("s_barrier" ::: "memory");
        else asm volatile("s_cmp_eq_u32 %0, 2\n\ts_cbranch_scc1 1f\n\ts_barrier\n1:" :: "s"(p) : "scc", "memory");
        SB0();
        mm18(1, wb, oa, [&](int m) {
          if (m == 9 || m == 10) oa[3 * (m & 1)] = rd_o2((j + 1) & 3, 3 * (m & 1));
          else if (m == 15 || m == 16) oa[3 * (m & 1) + 1] = rd_o2((j + 1) & 3, 3 * (m & 1) + 1);
          else if (m < 14) rd_w2h(wa, (j + 1) & 3, 0, m < 9 ? m : m - 2); });
        MEMFENCE();
      }
      store_pass(p);
      if (p == 0) unpark(pA); else unpark(pB);
    }
    WAITV(0);
    publish(2);
    await_pass(2, __builtin_amdgcn_readlane(tick, 2));
    const unsigned now = __builtin_amdgcn_raw_buffer_load_b32(rf, 4 * (lane < 2 ? 2 * u + lane : mem), 0, 16);
    sum_pass(std::integral_constant<int, 2>{}, 36, 2, 72 * u);
    if (now != tick) { tick = now; publish(0); publish(1); publish(2); }
  } else {
    const unsigned mine = __builtin_amdgcn_raw_buffer_load_b32(rf, 4 * mem, 0, 16);
#pragma clang loop unroll(disable)
    for (unsigned j = 0; j < 11; ++j) {
      if (j < 10) WAITV(12); else WAITV(0);
      BARRIER();
      if (j >= 1 && j < 9) {
        const unsigned k = 2 * j + 6, buf = ldsb + ((k & 4) ? 0 : WREG) + (k & 3) * WOU;
        wo_issue(24 + k, buf); wo_issue(25 + k, buf + WOU);
      }
    }
    await_pass(jj, mine);
    sum_pass(std::integral_constant<int, 3>{}, 48, jj, 144 * qh);
  }
  asm volatile("s_waitcnt vmcnt(0)\nattn_code_end:" ::: "memory");
}

extern "C" void kernel_launch(void* const* d_in, const int* in_sizes, int n_in, void* d_out, int out_size, void* d_ws, size_t ws_size,
                              hipStream_t stream) {
  (void)in_sizes; (void)n_in; (void)out_size; (void)ws_size;
  (void)hipFuncSetAttribute((const void*)attn_main, hipFuncAttributeMaxDynamicSharedMemorySize, LDS_MAIN);
  attn_main<<<256, 512, LDS_MAIN, stream>>>((const char*)d_in[0], (const char*)d_in[1], (const char*)d_in[2], (const char*)d_in[3], (const char*)d_in[4],
                                            (const char*)d_in[5], (const char*)d_in[6], (const char*)d_in[7], (const char*)d_in[8], (const char*)d_in[9], (char*)d_ws, (char*)d_out);
}
```

```cpp
#include <hip/hip_runtime.h>
#include <hip/hip_bf16.h>
#include <stdint.h>
#include <type_traits>

#define LAS __attribute__((address_space(3)))
#define DI __device__ __forceinline__
typedef __bf16 bf16x2 __attribute__((ext_vector_type(2)));
typedef __bf16 bf16x4 __attribute__((ext_vector_type(4)));
typedef __bf16 bf16x8 __attribute__((ext_vector_type(8)));
typedef float f32x2 __attribute__((ext_vector_type(2)));
typedef float f32x4 __attribute__((ext_vector_type(4)));
typedef unsigned u32x4 __attribute__((ext_vector_type(4)));
typedef unsigned u32x2 __attribute__((ext_vector_type(2)));

constexpr int NB = 16, SQ = 96, DM = 576, NH = 32, SK = 512, DH = 128, HD = NH * DH;
constexpr unsigned LDS_MAIN = 163840;
constexpr float SC_LOG2E = 0.08838834764831845f * 1.4426950408889634f;
constexpr float THR = 8.0f;

DI unsigned rfl(unsigned x) { return __builtin_amdgcn_readfirstlane(x); }
DI unsigned cvtpk(float lo, float hi) { f32x2 v = {lo, hi}; bf16x2 b = __builtin_convertvector(v, bf16x2); return __builtin_bit_cast(unsigned, b); }
DI bf16x8 pack2(const f32x4& a, const f32x4& b) {
  u32x4 w = {cvtpk(a[0], a[1]), cvtpk(a[2], a[3]), cvtpk(b[0], b[1]), cvtpk(b[2], b[3])};
  return __builtin_bit_cast(bf16x8, w);
}
DI bf16x8 pack_swap(const f32x4& a0, const f32x4& a1) {
  unsigned p00 = cvtpk(a0[0], a0[1]), p01 = cvtpk(a0[2], a0[3]), p10 = cvtpk(a1[0], a1[1]), p11 = cvtpk(a1[2], a1[3]);
  auto r0 = __builtin_amdgcn_permlane32_swap(p00, p10, false, false);
  auto r1 = __builtin_amdgcn_permlane32_swap(p01, p11, false, false);
  auto t0 = __builtin_amdgcn_permlane16_swap(r0[0], r0[1], false, false);
  auto t1 = __builtin_amdgcn_permlane16_swap(r1[0], r1[1], false, false);
  u32x4 w = {t0[0], t1[0], t0[1], t1[1]};
  return __builtin_bit_cast(bf16x8, w);
}
DI bf16x8 lds_b128(unsigned a) { return *(const LAS bf16x8*)(uintptr_t)a; }
DI bf16x8 lds_tr2(unsigned a0, unsigned a1) {
  bf16x4 lo = __builtin_amdgcn_ds_read_tr16_b64_v4bf16((LAS bf16x4*)(uintptr_t)a0);
  bf16x4 hi = __builtin_amdgcn_ds_read_tr16_b64_v4bf16((LAS bf16x4*)(uintptr_t)a1);
  return __builtin_shufflevector(lo, hi, 0, 1, 2, 3, 4, 5, 6, 7);
}
DI bf16x4 lds_tr1(unsigned a) { return __builtin_amdgcn_ds_read_tr16_b64_v4bf16((LAS bf16x4*)(uintptr_t)a); }
DI void set_half(bf16x8& f, const int hi, bf16x4 v) { f = hi ? __builtin_shufflevector(f, __builtin_shufflevector(v, v, 0, 1, 2, 3, 0, 1, 2, 3), 0, 1, 2, 3, 12, 13, 14, 15) : __builtin_shufflevector(v, v, 0, 1, 2, 3, -1, -1, -1, -1); }
DI float max2f(float a, float b) { return __builtin_elementwise_maximum(a, b); }
DI float max3f(float a, float b, float c) { return max2f(max2f(a, b), c); }
DI float xg_max(float v) {
  auto r = __builtin_amdgcn_permlane32_swap(__float_as_uint(v), __float_as_uint(v), false, false); v = max2f(__uint_as_float(r[0]), __uint_as_float(r[1]));
  auto s = __builtin_amdgcn_permlane16_swap(__float_as_uint(v), __float_as_uint(v), false, false); return max2f(__uint_as_float(s[0]), __uint_as_float(s[1]));
}
DI float xg_sum(float v) {
  auto r = __builtin_amdgcn_permlane32_swap(__float_as_uint(v), __float_as_uint(v), false, false); v = __uint_as_float(r[0]) + __uint_as_float(r[1]);
  auto s = __builtin_amdgcn_permlane16_swap(__float_as_uint(v), __float_as_uint(v), false, false); return __uint_as_float(s[0]) + __uint_as_float(s[1]);
}
#define MFMA16(a, b, c) __builtin_amdgcn_mfma_f32_16x16x32_bf16((a), (b), (c), 0, 0, 0)
#define SB0() __builtin_amdgcn_sched_barrier(0)
#define MEMFENCE() asm volatile("" ::: "memory")
#define BARRIER() do { SB0(); __builtin_amdgcn_s_waitcnt(0xC07F); asm volatile("s_barrier" ::: "memory"); SB0(); } while (0)
#define WAITV(n) asm volatile("s_waitcnt vmcnt(" #n ")" ::: "memory")
template <int N> DI void weave() {
#pragma unroll
  for (int i = 0; i < N; ++i) { __builtin_amdgcn_sched_group_barrier(0x8, 1, 0); __builtin_amdgcn_sched_group_barrier(0x100, 1, 0); }
}
template <int N> DI void weave2() {
#pragma unroll
  for (int i = 0; i < N; ++i) { __builtin_amdgcn_sched_group_barrier(0x8, 2, 0); __builtin_amdgcn_sched_group_barrier(0x100, 2, 0); }
}
DI __amdgpu_buffer_rsrc_t mkrsrc(const void* p, unsigned bytes) { return __builtin_amdgcn_make_buffer_rsrc((void*)p, (short)0, (int)bytes, 0x00020000); }
DI void dma4_nt(__amdgpu_buffer_rsrc_t r, unsigned lds, unsigned va, unsigned vb, unsigned so) {
  asm volatile("s_mov_b32 m0, %0\n\ts_nop 0\n\t"
               "buffer_load_dwordx4 %1, %3, %4 offen nt lds\n\t"
               "buffer_load_dwordx4 %1, %3, %4 offen offset:1024 nt lds\n\t"
               "buffer_load_dwordx4 %2, %3, %4 offen offset:2048 nt lds\n\t"
               "buffer_load_dwordx4 %2, %3, %4 offen offset:3072 nt lds"
               :: "s"(lds), "v"(va), "v"(vb), "s"(r), "s"(so) : "memory");
}
DI void dma4(__amdgpu_buffer_rsrc_t r, unsigned lds, unsigned v0, unsigned v1, unsigned v2, unsigned v3, unsigned so) {
  asm volatile("s_mov_b32 m0, %0\n\ts_nop 0\n\t"
               "buffer_load_dwordx4 %1, %5, %6 offen lds\n\t"
               "buffer_load_dwordx4 %2, %5, %6 offen offset:1024 lds\n\t"
               "buffer_load_dwordx4 %3, %5, %6 offen offset:2048 lds\n\t"
               "buffer_load_dwordx4 %4, %5, %6 offen offset:3072 lds"
               :: "s"(lds), "v"(v0), "v"(v1), "v"(v2), "v"(v3), "s"(r), "s"(so) : "memory");
}
DI void dma3(__amdgpu_buffer_rsrc_t r, unsigned lds, unsigned v0, unsigned v1, unsigned v2, unsigned so) {
  asm volatile("s_mov_b32 m0, %0\n\ts_nop 0\n\t"
               "buffer_load_dwordx4 %1, %4, %5 offen lds\n\t"
               "buffer_load_dwordx4 %2, %4, %5 offen offset:1024 lds\n\t"
               "buffer_load_dwordx4 %3, %4, %5 offen offset:2048 lds"
               :: "s"(lds), "v"(v0), "v"(v1), "v"(v2), "s"(r), "s"(so) : "memory");
}
DI void dma3w(__amdgpu_buffer_rsrc_t r, unsigned lds, unsigned v0, unsigned v1, unsigned v2, unsigned so) {
  asm volatile("s_mov_b32 m0, %0\n\ts_nop 3\n\t"
               "buffer_load_dwordx4 %1, %4, %5 offen lds\n\t"
               "buffer_load_dwordx4 %2, %4, %5 offen offset:1024 lds\n\t"
               "buffer_load_dwordx4 %3, %4, %5 offen offset:2048 lds"
               :: "s"(lds), "v"(v0), "v"(v1), "v"(v2), "s"(r), "s"(so) : "memory");
}
DI void touch(__amdgpu_buffer_rsrc_t r, unsigned lds, unsigned v, unsigned so) {
  asm volatile("s_mov_b32 m0, %0\n\ts_nop 0\n\tbuffer_load_dword %1, %2, %3 offen lds" :: "s"(lds), "v"(v), "s"(r), "s"(so) : "memory");
}

#define KVREC (4 * SK * DH * 2)
#define CODEPF 1
#define TLBPF 1
constexpr unsigned KVS = 16384, NKV = 4;
constexpr unsigned QB1 = 73728, QB2 = 118784;
constexpr unsigned SCR = 65536;
constexpr unsigned OREG = 65536, WREG = 114688, WOU = 12288;
constexpr unsigned FLG = 2048;
constexpr unsigned PART = 110592;

__global__ __launch_bounds__(512, 2) void attn_main(const char* xA, const char* kA, const char* vA, const char* wqA, const char* woA,
                                                    const char* xB, const char* kB, const char* vB, const char* wqB, const char* woB, char* ows, char* out) {
  extern __shared__ __attribute__((aligned(16))) char lds[];
  const unsigned ldsb = rfl((unsigned)(uintptr_t)(LAS char*)lds);
  const unsigned wid = rfl(threadIdx.x >> 6), lane = threadIdx.x & 63, team = wid >> 2, u = wid & 3, jj = u >> 1, qh = u & 1;
  const unsigned id = blockIdx.x, xcd = id & 7, idx = id >> 3, blk = xcd >> 2;
  const unsigned b = idx & 15, hb = 8 * (xcd & 3) + 4 * (idx >> 4), h = hb + 2 * team + jj;
  const char* kp = blk ? kB : kA; const char* vp = blk ? vB : vA; const char* wop = blk ? woB : woA;
  const __amdgpu_buffer_rsrc_t rk = mkrsrc(kp + (size_t)(b * NH + hb) * SK * DH * 2, KVREC);
  const __amdgpu_buffer_rsrc_t rv = mkrsrc(vp + (size_t)(b * NH + hb) * SK * DH * 2, KVREC);
  const __amdgpu_buffer_rsrc_t ro = mkrsrc(wop, HD * DM * 2);
  const unsigned grp = blk * NB + b, mem = hb >> 2;
  const __amdgpu_buffer_rsrc_t rf = mkrsrc(ows + (size_t)256 * PART + grp * FLG, FLG);
  const unsigned g = lane >> 4, c16 = lane & 15, q4 = c16 >> 2, p4 = lane & 3;
  const unsigned vka = g * 256 + 16 * (c16 ^ ((8 * qh) | g)), vkb = g * 256 + 16 * (c16 ^ ((8 * qh + 4) | g));
  const unsigned vva = g * 256 + 16 * (c16 ^ (g << 2)), vvb = g * 256 + 16 * (c16 ^ ((g << 2) | 2));
  const unsigned krot = (id * 5 + (id >> 3) + jj * 7) & 15;
  const unsigned kvs0 = jj * (SK * DH * 2) + 4096 * qh, kvd0 = ldsb + 8192 * jj + 4096 * qh;
  auto kv_so = [&](unsigned T) { return kvs0 + ((T & 16) << 14) + (((T + krot) & 15) << 13); };
  auto k_issue = [&](unsigned T) { dma4_nt(rk, kvd0 + 3 * KVS - (T & 1) * (2 * KVS), vka, vkb, kv_so(T)); };
  auto v_issue = [&](unsigned T) { dma4_nt(rv, kvd0 + (T & 1) * (2 * KVS), vva, vvb, kv_so(T)); };
  const unsigned tro = 256 * (8 * g + q4) + 16 * ((q4 << 2) | ((g & 1) << 1) | (p4 >> 1)) + 8 * (p4 & 1);
  const unsigned xo0 = 32768 + (48 * qh + c16) * 128 + 16 * (g ^ ((c16 >> 1) & 7));
  auto rd_tr8 = [&](bf16x8 (&f)[8], unsigned a, unsigned imm) {
#pragma unroll
    for (int d = 0; d < 8; ++d) f[d] = lds_tr2((a ^ (32 * d)) + imm, (a ^ (32 * d)) + imm + 1024);
  };

  bf16x8 qf[4][3];
  f32x4 acc[8][3];
  auto zero_acc = [&]() {
#pragma unroll
    for (int d = 0; d < 8; ++d)
#pragma unroll
      for (int q = 0; q < 3; ++q) acc[d][q] = f32x4{};
  };
  auto mm24 = [&](const int hf, const bf16x8 (&w)[8], bf16x8 (&x)[6], auto&& fill) {
#pragma unroll
    for (int m = 0; m < 24; ++m) {
      const int q = m >> 3, i = (m >> 1) & 3, ks = m & 1;
      acc[4 * hf + i][q] = MFMA16(w[2 * i + ks], x[3 * ks + q], acc[4 * hf + i][q]);
      fill(m);
      SB0();
    }
  };
  auto nofill = [](int) {};
  auto rd_w1 = [&](const int hf, const int i, unsigned a) { const unsigned p = (a ^ (128 * hf + 32 * (i >> 1))) + 8192 * (i & 1); return lds_tr2(p, p + 1024); };
  auto rd_wh = [&](bf16x8 (&w)[8], const int hf, const int r, unsigned a) { const int i = r >> 1; set_half(w[i], r & 1, lds_tr1((a ^ (128 * hf + 32 * (i >> 1))) + 8192 * (i & 1) + 1024 * (r & 1))); };
  auto rd_x1 = [&](const int i, unsigned xo) { return lds_b128((xo ^ (64 * (i / 3))) + 2048 * (i % 3)); };
  auto rd_w = [&](const int hf, bf16x8 (&w)[8], unsigned a) {
#pragma unroll
    for (int i = 0; i < 8; ++i) { const unsigned p = (a ^ (128 * hf + 32 * (i >> 1))) + 8192 * (i & 1); w[i] = lds_tr2(p, p + 1024); }
  };
  auto rd_x = [&](bf16x8 (&x)[6], unsigned xo) {
#pragma unroll
    for (int i = 0; i < 6; ++i) x[i] = lds_b128((xo ^ (64 * (i / 3))) + 2048 * (i % 3));
  };
  auto pack_q = [&]() {
#pragma unroll
    for (int s = 0; s < 4; ++s)
#pragma unroll
      for (int q = 0; q < 3; ++q) qf[s][q] = pack_swap(acc[2 * s][q] * SC_LOG2E, acc[2 * s + 1][q] * SC_LOG2E);
  };

  unsigned long long pc0; unsigned clen;
  asm volatile("attn_code_begin:\n\ts_getpc_b64 %0\n\ts_mov_b32 %1, attn_code_end-attn_code_begin-8" : "=s"(pc0), "=s"(clen));
  if (team == 0) {
    if (u == 1) touch(mkrsrc((const void*)pc0, clen), ldsb + SCR + 256, (idx * 8 + (lane & 7)) * 128, 0);
    if (u == 2) { touch(rk, ldsb + SCR + 512, (lane & 3) * 131072, 0); touch(rv, ldsb + SCR + 512, (lane & 3) * 131072, 0); }
    if (u == 3) { touch(ro, ldsb + SCR + 512, 0, hb * DH * (DM * 2)); touch(mkrsrc(ows + (size_t)grp * 8 * PART, 8 * PART), ldsb + SCR + 512, (lane & 7) * PART, 0); touch(mkrsrc(out + (size_t)b * SQ * 2 * DM * 2, 4096), ldsb + SCR + 512, 0, 0); }
    if (u == 0) {
      const unsigned old = __builtin_amdgcn_raw_buffer_load_b32(rf, lane < 24 ? 128 * (1 + mem) + 4 * lane : 4 * mem, 0, 16);
      const unsigned base = __builtin_amdgcn_readlane(old, 24) + 1;
      unsigned k = 0;
      while (__any(old - base == k)) ++k;
      if (lane == 0) __builtin_amdgcn_raw_buffer_store_b32(base + k, rf, 4 * mem, 0, 16);
    }
    zero_acc();
    bf16x8 wa[8], wb[8], xa[6];
#pragma unroll
    for (int d = 0; d < 8; ++d) wb[d] = bf16x8{};
#pragma unroll
    for (int q = 0; q < 6; ++q) xa[q] = bf16x8{};
    unsigned bufb = ldsb;
#pragma clang loop unroll(disable)
    for (int c = 0; c < 9; ++c) {
      const unsigned a = bufb + 16384 * jj + tro, xo = bufb + xo0;
      bufb = bufb == ldsb ? ldsb + QB1 : bufb == ldsb + QB1 ? ldsb + QB2 : ldsb;
      BARRIER();
      mm24(1, wb, xa, [&](int m) {
        if (m == 12 || m == 13) xa[3 * (m & 1)] = rd_x1(3 * (m & 1), xo);
        else if (m == 20 || m == 21) xa[3 * (m & 1) + 1] = rd_x1(3 * (m & 1) + 1, xo);
        else if (m < 18) rd_wh(wa, 0, m < 12 ? m : m - 2, a); });
      mm24(0, wa, xa, [&](int m) { if (m == 4 || m == 5) xa[3 * (m & 1) + 2] = rd_x1(3 * (m & 1) + 2, xo); else if (m < 18) rd_wh(wb, 1, m < 4 ? m : m - 2, a); });
    }
    mm24(1, wb, xa, nofill);
    pack_q();
  } else {
    const char* xp = blk ? xB : xA; const char* wp = blk ? wqB : wqA;
    const __amdgpu_buffer_rsrc_t rx = mkrsrc(xp + (size_t)b * SQ * DM * 2, SQ * DM * 2);
    const __amdgpu_buffer_rsrc_t rw = mkrsrc(wp, DM * HD * 2);
    unsigned vw[4], vx[3];
#pragma unroll
    for (int pp = 0; pp < 4; ++pp) vw[pp] = (4 * pp + g) * (HD * 2) - 1024 * pp + 16 * (c16 ^ ((g << 2) | (pp & 2)));
#pragma unroll
    for (int i = 0; i < 3; ++i) vx[i] = (8 * i + (lane >> 3)) * (DM * 2) - 1024 * i + 16 * ((lane & 7) ^ ((4 * (3 * u + i) + g) & 7));
    const unsigned qrot = (b + 4 * (idx >> 4)) % 9;
    auto x_issue = [&](unsigned c, unsigned buf) { dma3(rx, buf + 32768 + 3072 * u, vx[0], vx[1], vx[2], c * 128 + 24 * u * (DM * 2)); };
    auto w_issue = [&](unsigned c, unsigned ks, unsigned buf, unsigned head) {
      dma4(rw, buf + 16384 * jj + 8192 * ks + 4096 * qh, vw[0], vw[1], vw[2], vw[3], (c * 64 + 32 * ks + 16 * qh) * (HD * 2) + head * 256);
    };
    auto nxt = [&](unsigned c) { return c == 8 ? 0u : c + 1; };
    unsigned cq = qrot;
    x_issue(cq, ldsb); w_issue(cq, 0, ldsb, h - 2); w_issue(cq, 1, ldsb, h - 2); cq = nxt(cq);
    x_issue(cq, ldsb + QB1); w_issue(cq, 0, ldsb + QB1, h - 2); w_issue(cq, 1, ldsb + QB1, h - 2); cq = nxt(cq);
    { unsigned c = 2 + (lane >= 24) + (lane >= 48) + qrot; if (c >= 9) c -= 9;
      touch(rx, ldsb + SCR, (24 * u + lane % 24) * (DM * 2) + c * 128, 0);
      c = 4 + qrot; if (c >= 9) c -= 9;
      touch(rx, ldsb + SCR, (24 * u + 16 + (lane & 7)) * (DM * 2) + c * 128, 0); }
    unsigned bufb = ldsb + QB2;
    WAITV(13);
#pragma clang loop unroll(disable)
    for (int c = 0; c < 7; ++c) {
      BARRIER();
      x_issue(cq, bufb); w_issue(cq, 0, bufb, h - 2); w_issue(cq, 1, bufb, h - 2); cq = nxt(cq);
      bufb = bufb == ldsb ? ldsb + QB1 : bufb == ldsb + QB1 ? ldsb + QB2 : ldsb;
      WAITV(11);
    }
    k_issue(0);
    BARRIER();
    v_issue(0);
    WAITV(8); BARRIER();
    k_issue(1); v_issue(1);
    zero_acc();
    WAITV(12); BARRIER();
    x_issue(cq, ldsb + QB1); w_issue(cq, 0, ldsb + QB1, h); w_issue(cq, 1, ldsb + QB1, h); cq = nxt(cq);
    WAITV(19); BARRIER();
    k_issue(2);
    unsigned T = 1;
#pragma clang loop unroll(disable)
    for (int c = 0; c < 9; ++c) {
      const unsigned qb = ldsb + ((c & 1) ? QB2 : QB1), qn = ldsb + ((c & 1) ? QB1 : QB2);
      const bool fast = c & 1;
      bf16x8 wf[2][8], xf[6];
      if (c == 0) WAITV(4); else WAITV(8);
      BARRIER();
      rd_x(xf, qb + xo0);
      rd_w(0, wf[0], qb + 16384 * jj + tro);
      if (!fast) {
        if (c < 8) { x_issue(cq, qn); w_issue(cq, 0, qn, h); }
        v_issue(T + 1);
        if (c < 8) WAITV(15); else WAITV(8);
        BARRIER();
        k_issue(T + 2); ++T;
        mm24(0, wf[0], xf, nofill);
        if (c < 8) WAITV(15); else WAITV(8);
        BARRIER();
      } else { x_issue(cq, qn); w_issue(cq, 0, qn, h); }
      rd_w(1, wf[1], qb + 16384 * jj + tro);
      if (c < 8) w_issue(cq, 1, qn, h);
      cq = nxt(cq);
      v_issue(T + 1);
      if (fast) WAITV(19); else if (c < 8) WAITV(12); else WAITV(8);
      BARRIER();
      k_issue(T + 2); ++T;
      if (fast) mm24(0, wf[0], xf, nofill);
      mm24(1, wf[1], xf, nofill);
    }
    WAITV(8); BARRIER();
    v_issue(16);
    pack_q();
    WAITV(8); BARRIER();
    k_issue(17);
  }

  const unsigned prot = (b >> 3) + (b & 1), ksrot = b & 7;
  unsigned vo[3];
#pragma unroll
  for (int i = 0; i < 3; ++i) { const unsigned L = 64 * i + lane, row = L / 24, ph = L - 24 * row; vo[i] = row * (DM * 2) + 16 * (ph ^ (row & 6)) - 1024 * i; }
  unsigned wo_tab;
  { const unsigned r = lane >= 24, k = lane - 24 * r; unsigned p = ((k >> 3) + prot + 2 * r) % 3;
    wo_tab = ((hb + 2 * r) * DH + 32 * ((k + ksrot) & 7) + 8 * u) * (DM * 2) + 384 * p; }
  auto wo_issue = [&](unsigned i, unsigned buf) { dma3w(ro, buf + 3072 * u, vo[0], vo[1], vo[2], __builtin_amdgcn_readlane(wo_tab, i)); };

  {
    zero_acc();
    f32x4 negm[3] = {f32x4{}, f32x4{}, f32x4{}};
    f32x2 l2[3] = {f32x2{}, f32x2{}, f32x2{}};
    const unsigned koff = ldsb + KVS + 8192 * jj + 256 * (8 * q4 + p4) + 16 * (g ^ c16), voff = ldsb + 8192 * jj + tro;
    f32x4 sc[2][3];
    bf16x8 kf[2][4], vf[8], pf[3];
    auto qk = [&]() {
#pragma unroll
      for (int kb = 0; kb < 2; ++kb)
#pragma unroll
        for (int q = 0; q < 3; ++q) {
          sc[kb][q] = MFMA16(kf[kb][0], qf[0][q], negm[q]);
#pragma unroll
          for (int s = 1; s < 4; ++s) sc[kb][q] = MFMA16(kf[kb][s], qf[s][q], sc[kb][q]);
          SB0();
        }
    };
    WAITV(4);
    BARRIER();
#pragma unroll
    for (int s = 0; s < 4; ++s) { const unsigned a = (2 * KVS + koff) ^ (64 * s); kf[0][s] = lds_b128(a); kf[1][s] = lds_b128(a + 1024); }
    qk();
#pragma clang loop unroll(disable)
    for (int t = 0; t < 16; ++t) {
      const unsigned sb = (t & 1) * (2 * KVS);
      BARRIER();
      rd_tr8(vf, sb + voff, 0);
      MEMFENCE(); SB0();
      float mx[3];
#pragma unroll
      for (int q = 0; q < 3; ++q)
        mx[q] = max3f(max3f(sc[0][q][0], sc[0][q][1], sc[0][q][2]), max3f(sc[0][q][3], sc[1][q][0], sc[1][q][1]), max2f(sc[1][q][2], sc[1][q][3]));
      if (t == 0 || __builtin_expect(__any(!(max3f(mx[0], mx[1], mx[2]) <= THR)), 0)) {
#pragma unroll
        for (int q = 0; q < 3; ++q) {
          float dl = xg_max(mx[q]);
          if (t != 0) dl = max2f(dl, 0.f);
          negm[q] -= dl; sc[0][q] -= dl; sc[1][q] -= dl; mx[q] = dl;
        }
        if (t != 0) {
#pragma unroll
          for (int q = 0; q < 3; ++q) {
            const float al = __builtin_amdgcn_exp2f(-mx[q]);
            l2[q] *= al;
#pragma unroll
            for (int d = 0; d < 8; ++d) acc[d][q] *= al;
          }
        }
      }
#pragma unroll
      for (int q = 0; q < 3; ++q) {
#pragma unroll
        for (int i = 0; i < 4; ++i) { sc[0][q][i] = __builtin_amdgcn_exp2f(sc[0][q][i]); sc[1][q][i] = __builtin_amdgcn_exp2f(sc[1][q][i]); }
        { const f32x4 s4 = sc[0][q] + sc[1][q]; l2[q] += f32x2{s4[0], s4[1]} + f32x2{s4[2], s4[3]}; }
        pf[q] = pack2(sc[0][q], sc[1][q]);
      }
      asm volatile("" : "+v"(pf[0]), "+v"(pf[1]), "+v"(pf[2]), "+v"(l2[0]), "+v"(l2[1]), "+v"(l2[2]));
      WAITV(0); BARRIER();
      if (t == 15 && team == 0) { wo_issue(0, ldsb + WREG); wo_issue(1, ldsb + WREG + WOU); v_issue(17); }
#pragma unroll
      for (int d = 0; d < 8; ++d) {
#pragma unroll
        for (int q = 0; q < 3; ++q) acc[d][q] = MFMA16(vf[d], pf[q], acc[d][q]);
        if (d >= 2 && d < 6) { const int kb = (d - 2) >> 1, s = (d & 1) * 2; kf[kb][s] = lds_b128(((sb + koff) ^ (64 * s)) + 1024 * kb); kf[kb][s + 1] = lds_b128(((sb + koff) ^ (64 * s + 64)) + 1024 * kb); MEMFENCE(); }
        SB0();
      }
      if (t < 15) qk();
    }
    if (team == 0) { BARRIER(); wo_issue(2, ldsb + WREG + 2 * WOU); k_issue(18); }
#pragma unroll
    for (int q = 0; q < 3; ++q) {
      const float inv = __builtin_amdgcn_rcpf(xg_sum(l2[q][0] + l2[q][1]));
#pragma unroll
      for (int s = 0; s < 4; ++s)
        *(LAS bf16x8*)(uintptr_t)(ldsb + OREG + (((4 * jj + s - ksrot) & 7) * 6 + 3 * qh + q) * 1024 + lane * 16) = pack2(acc[2 * s][q] * inv, acc[2 * s + 1][q] * inv);
    }
  }

  const __amdgpu_buffer_rsrc_t rg = mkrsrc(ows + (size_t)grp * 8 * PART, 8 * PART);
  auto await_pass = [&](unsigned pi, unsigned mine) {
#pragma clang loop unroll(disable)
    for (unsigned spin = 0; spin < (1u << 22); ++spin) {
      const unsigned ec = __builtin_amdgcn_raw_buffer_load_b32(rf, 128 * (1 + mem) + 4 * (lane & 7), 32 * pi, 16);
      MEMFENCE();
      if (__all(ec == mine)) break;
      __builtin_amdgcn_s_sleep(1);
    }
  };
  auto sum_pass = [&](auto NS_, unsigned NL, unsigned pi, unsigned r1) {
    constexpr int NS = NS_.value;
    const unsigned p = (pi + prot + 2) % 3;
    if (lane < NL) {
      const unsigned r0 = mem * 288 + r1 + lane;
      u32x4 v[NS][8];
#pragma unroll
      for (int s = 0; s < NS; ++s)
#pragma unroll
        for (int i = 0; i < 8; ++i) v[s][i] = __builtin_amdgcn_raw_buffer_load_b128(rg, (r0 + NL * s) * 16, i * PART + p * 36864, 16);
#pragma unroll
      for (int s = 0; s < NS; ++s) {
        float a[8] = {0.f, 0.f, 0.f, 0.f, 0.f, 0.f, 0.f, 0.f};
#pragma unroll
        for (int i = 0; i < 8; ++i)
#pragma unroll
          for (int k = 0; k < 4; ++k) { a[2 * k] += __uint_as_float(v[s][i][k] << 16); a[2 * k + 1] += __uint_as_float(v[s][i][k] & 0xffff0000u); }
        const unsigned r = r0 + NL * s, ln = r & 63, f = r >> 6, uu = f / 9, i9 = f - 9 * uu, np = i9 / 3, q = i9 - 3 * np;
        const unsigned row = 48 * (uu & 1) + 16 * q + (ln & 15), col = 192 * p + 96 * (uu >> 1) + 32 * np + 4 * (ln >> 4);
        char* op = out + ((size_t)(b * SQ + row) * (2 * DM) + blk * DM + col) * 2;
        *(u32x2*)op = u32x2{cvtpk(a[0], a[1]), cvtpk(a[2], a[3])};
        *(u32x2*)(op + 32) = u32x2{cvtpk(a[4], a[5]), cvtpk(a[6], a[7])};
      }
    }
  };

  if (team == 0) {
    unsigned wof[6];
#pragma unroll
    for (int n = 0; n < 6; ++n) wof[n] = ldsb + (4 * g + q4) * 384 + 16 * ((12 * jj + 2 * n + (p4 >> 1)) ^ ((4 * g + q4) & 6)) + 8 * (p4 & 1);
    const unsigned oof = ldsb + OREG + 3 * qh * 1024 + lane * 16;
    f32x4 oc[6][3];
    unsigned pA[36], pB[36];
#pragma unroll
    for (int i = 0; i < 36; ++i) { pA[i] = 0; pB[i] = 0; }
    unsigned T = 17;
#pragma clang loop unroll(disable)
    for (int p = 0; p < 3; ++p) {
#pragma unroll
      for (int n = 0; n < 6; ++n)
#pragma unroll
        for (int q = 0; q < 3; ++q) oc[n][q] = f32x4{};
#pragma clang loop unroll(disable)
      for (unsigned k = 0; k < 8; k += 2, ++T) {
        WAITV(11); BARRIER();
        bf16x8 of[2][3], wf[2][6];
        const unsigned wb = WREG + (k & 2) * WOU, ob = oof + k * 6144;
#pragma unroll
        for (int i = 0; i < 2; ++i) {
#pragma unroll
          for (int q = 0; q < 3; ++q) of[i][q] = lds_b128(ob + 6144 * i + 1024 * q);
#pragma unroll
          for (int n = 0; n < 6; ++n) wf[i][n] = lds_tr2(wb + wof[n] + WOU * i, wb + wof[n] + WOU * i + 16 * 384);
        }
        wo_issue(2 * T - 31, ldsb + WREG + ((k & 2) ^ 3) * WOU);
        v_issue(T + 1);
        WAITV(14); BARRIER();
        wo_issue(2 * T - 30, ldsb + wb);
        k_issue(T + 2);
#pragma unroll
        for (int n = 0; n < 6; ++n)
#pragma unroll
          for (int q = 0; q < 3; ++q) { oc[n][q] = MFMA16(wf[0][n], of[0][q], oc[n][q]); oc[n][q] = MFMA16(wf[1][n], of[1][q], oc[n][q]); SB0(); }
      }
      if (p == 0) {
#pragma unroll
        for (int i = 0; i < 36; ++i) { const f32x4 v = oc[i / 6][(i / 2) % 3]; pA[i] = cvtpk(v[2 * (i & 1)], v[2 * (i & 1) + 1]); }
      } else if (p == 1) {
#pragma unroll
        for (int i = 0; i < 36; ++i) { const f32x4 v = oc[i / 6][(i / 2) % 3]; pB[i] = cvtpk(v[2 * (i & 1)], v[2 * (i & 1) + 1]); }
      }
    }
    WAITV(14); BARRIER(); wo_issue(27, ldsb + WREG + 3 * WOU); v_issue(30);
    WAITV(14); BARRIER(); k_issue(31);
    WAITV(11); BARRIER(); v_issue(31);
    auto unpark = [&](const unsigned (&p)[36]) {
#pragma unroll
      for (int i = 0; i < 18; ++i) {
        unsigned v0 = p[2 * i], v1 = p[2 * i + 1]; asm volatile("" : "+v"(v0), "+v"(v1));
        oc[i / 3][i % 3] = f32x4{__uint_as_float(v0 << 16), __uint_as_float(v0 & 0xffff0000u), __uint_as_float(v1 << 16), __uint_as_float(v1 & 0xffff0000u)};
      }
    };
    WAITV(8); BARRIER();
    unsigned woh[6];
#pragma unroll
    for (int n = 0; n < 6; ++n) { woh[n] = wof[n] + WREG; asm volatile("" : "+v"(woh[n])); }
    bf16x8 wa[6], wb[6], oa[6];
    WAITV(4); BARRIER();
    WAITV(0); BARRIER();
    wo_issue(28, ldsb); wo_issue(29, ldsb + WOU);
    BARRIER();
    wo_issue(30, ldsb + 2 * WOU); wo_issue(31, ldsb + 3 * WOU);
    const __amdgpu_buffer_rsrc_t rp = mkrsrc(ows + (size_t)(grp * 8 + mem) * PART, PART);
    unsigned tick = __builtin_amdgcn_raw_buffer_load_b32(rf, 4 * (lane < 2 ? 2 * u + lane : mem), 0, 16);
    auto publish = [&](unsigned pi) { __builtin_amdgcn_raw_buffer_store_b32(tick, rf, lane < 2 ? 128 * (1 + 2 * u + lane) + 4 * mem : FLG, 32 * pi, 16); };
    auto store_pass = [&](unsigned pi) {
      const unsigned p = (pi + prot + 2) % 3;
#pragma unroll
      for (int n = 0; n < 3; ++n)
#pragma unroll
        for (int q = 0; q < 3; ++q) __builtin_amdgcn_raw_buffer_store_b128(__builtin_bit_cast(u32x4, pack2(oc[2 * n][q], oc[2 * n + 1][q])), rp, lane * 16, p * 36864 + u * 9216 + (n * 3 + q) * 1024, 16);
    };
    auto rd_w2 = [&](const int j, const int hf, const int i) { const int k = 2 * j + (i & 1), n = 3 * hf + (i >> 1); const unsigned a = (k < 4 ? woh[n] : wof[n]) + (k & 3) * WOU; return lds_tr2(a, a + 16 * 384); };
    auto rd_o2 = [&](const int j, const int i) { return lds_b128(oof + (2 * j + i / 3) * 6144 + 1024 * (i % 3)); };
    auto rd_w2h = [&](bf16x8 (&w)[6], const int j, const int hf, const int r) {
      const int i = r >> 1, k = 2 * j + (i & 1), n = 3 * hf + (i >> 1);
      set_half(w[i], r & 1, lds_tr1((k < 4 ? woh[n] : wof[n]) + (k & 3) * WOU + 16 * 384 * (r & 1)));
    };
    auto mm18 = [&](const int hf, const bf16x8 (&w)[6], bf16x8 (&o)[6], auto&& fill) {
#pragma unroll
      for (int m = 0; m < 18; ++m) {
        const int q = m / 6, i = (m % 6) >> 1, un = m & 1;
        oc[3 * hf + i][q] = MFMA16(w[2 * i + un], o[3 * un + q], oc[3 * hf + i][q]);
        fill(m);
        SB0();
      }
    };
    BARRIER();
#pragma unroll
    for (int i = 0; i < 6; ++i) { oa[i] = rd_o2(0, i); wa[i] = rd_w2(0, 0, i); }
#pragma clang loop unroll(disable)
    for (unsigned p = 0; p < 3; ++p) {
#pragma unroll
      for (int j = 0; j < 4; ++j) {
        mm18(0, wa, oa, [&](int m) { if (m == 3 || m == 4) oa[3 * (m & 1) + 2] = rd_o2(j, 3 * (m & 1) + 2); else if (m < 14) rd_w2h(wb, j, 1, m < 3 ? m : m - 2); });
        if (j == 1) { WAITV(0); publish(p > 0 ? p - 1 : 64); }
        __builtin_amdgcn_s_waitcnt(0xC07F);
        if (j < 2) asm volatile("s_barrier" ::: "memory");
        else asm volatile("s_cmp_eq_u32 %0, 2\n\ts_cbranch_scc1 1f\n\ts_barrier\n1:" :: "s"(p) : "scc", "memory");
        SB0();
        mm18(1, wb, oa, [&](int m) {
          if (m == 9 || m == 10) oa[3 * (m & 1)] = rd_o2((j + 1) & 3, 3 * (m & 1));
          else if (m == 15 || m == 16) oa[3 * (m & 1) + 1] = rd_o2((j + 1) & 3, 3 * (m & 1) + 1);
          else if (m < 14) rd_w2h(wa, (j + 1) & 3, 0, m < 9 ? m : m - 2); });
        MEMFENCE();
      }
      store_pass(p);
      if (p == 0) unpark(pA); else unpark(pB);
    }
    WAITV(0);
    publish(2);
    await_pass(2, __builtin_amdgcn_readlane(tick, 2));
    const unsigned now = __builtin_amdgcn_raw_buffer_load_b32(rf, 4 * (lane < 2 ? 2 * u + lane : mem), 0, 16);
    sum_pass(std::integral_constant<int, 2>{}, 36, 2, 72 * u);
    if (now != tick) { tick = now; publish(0); publish(1); publish(2); }
  } else {
    const unsigned mine = __builtin_amdgcn_raw_buffer_load_b32(rf, 4 * mem, 0, 16);
#pragma clang loop unroll(disable)
    for (unsigned j = 0; j < 11; ++j) {
      if (j < 10) WAITV(12); else WAITV(0);
      BARRIER();
      if (j >= 1 && j < 9) {
        const unsigned k = 2 * j + 6, buf = ldsb + ((k & 4) ? 0 : WREG) + (k & 3) * WOU;
        wo_issue(24 + k, buf); wo_issue(25 + k, buf + WOU);
      }
    }
    await_pass(jj, mine);
    sum_pass(std::integral_constant<int, 3>{}, 48, jj, 144 * qh);
  }
  asm volatile("s_waitcnt vmcnt(0)\nattn_code_end:" ::: "memory");
}

extern "C" void kernel_launch(void* const* d_in, const int* in_sizes, int n_in, void* d_out, int out_size, void* d_ws, size_t ws_size,
                              hipStream_t stream) {
  (void)in_sizes; (void)n_in; (void)out_size; (void)ws_size;
  (void)hipFuncSetAttribute((const void*)attn_main, hipFuncAttributeMaxDynamicSharedMemorySize, LDS_MAIN);
  attn_main<<<256, 512, LDS_MAIN, stream>>>((const char*)d_in[0], (const char*)d_in[1], (const char*)d_in[2], (const char*)d_in[3], (const char*)d_in[4],
                                            (const char*)d_in[5], (const char*)d_in[6], (const char*)d_in[7], (const char*)d_in[8], (const char*)d_in[9], (char*)d_ws, (char*)d_out);
}
```

```cpp
#include <hip/hip_runtime.h>
#include <hip/hip_bf16.h>
#include <stdint.h>
#include <type_traits>

#define LAS __attribute__((address_space(3)))
#define DI __device__ __forceinline__
typedef __bf16 bf16x2 __attribute__((ext_vector_type(2)));
typedef __bf16 bf16x4 __attribute__((ext_vector_type(4)));
typedef __bf16 bf16x8 __attribute__((ext_vector_type(8)));
typedef float f32x2 __attribute__((ext_vector_type(2)));
typedef float f32x4 __attribute__((ext_vector_type(4)));
typedef unsigned u32x4 __attribute__((ext_vector_type(4)));
typedef unsigned u32x2 __attribute__((ext_vector_type(2)));

constexpr int NB = 16, SQ = 96, DM = 576, NH = 32, SK = 512, DH = 128, HD = NH * DH;
constexpr unsigned LDS_MAIN = 163840;
constexpr float SC_LOG2E = 0.08838834764831845f * 1.4426950408889634f;
constexpr float THR = 8.0f;

DI unsigned rfl(unsigned x) { return __builtin_amdgcn_readfirstlane(x); }
DI unsigned cvtpk(float lo, float hi) { f32x2 v = {lo, hi}; bf16x2 b = __builtin_convertvector(v, bf16x2); return __builtin_bit_cast(unsigned, b); }
DI bf16x8 pack2(const f32x4& a, const f32x4& b) {
  u32x4 w = {cvtpk(a[0], a[1]), cvtpk(a[2], a[3]), cvtpk(b[0], b[1]), cvtpk(b[2], b[3])};
  return __builtin_bit_cast(bf16x8, w);
}
DI bf16x8 pack_swap(const f32x4& a0, const f32x4& a1) {
  unsigned p00 = cvtpk(a0[0], a0[1]), p01 = cvtpk(a0[2], a0[3]), p10 = cvtpk(a1[0], a1[1]), p11 = cvtpk(a1[2], a1[3]);
  auto r0 = __builtin_amdgcn_permlane32_swap(p00, p10, false, false);
  auto r1 = __builtin_amdgcn_permlane32_swap(p01, p11, false, false);
  auto t0 = __builtin_amdgcn_permlane16_swap(r0[0], r0[1], false, false);
  auto t1 = __builtin_amdgcn_permlane16_swap(r1[0], r1[1], false, false);
  u32x4 w = {t0[0], t1[0], t0[1], t1[1]};
  return __builtin_bit_cast(bf16x8, w);
}
DI bf16x8 lds_b128(unsigned a) { return *(const LAS bf16x8*)(uintptr_t)a; }
DI bf16x8 lds_tr2(unsigned a0, unsigned a1) {
  bf16x4 lo = __builtin_amdgcn_ds_read_tr16_b64_v4bf16((LAS bf16x4*)(uintptr_t)a0);
  bf16x4 hi = __builtin_amdgcn_ds_read_tr16_b64_v4bf16((LAS bf16x4*)(uintptr_t)a1);
  return __builtin_shufflevector(lo, hi, 0, 1, 2, 3, 4, 5, 6, 7);
}
DI bf16x4 lds_tr1(unsigned a) { return __builtin_amdgcn_ds_read_tr16_b64_v4bf16((LAS bf16x4*)(uintptr_t)a); }
DI void set_half(bf16x8& f, const int hi, bf16x4 v) { f = hi ? __builtin_shufflevector(f, __builtin_shufflevector(v, v, 0, 1, 2, 3, 0, 1, 2, 3), 0, 1, 2, 3, 12, 13, 14, 15) : __builtin_shufflevector(v, v, 0, 1, 2, 3, -1, -1, -1, -1); }
DI float max2f(float a, float b) { return __builtin_elementwise_maximum(a, b); }
DI float max3f(float a, float b, float c) { return max2f(max2f(a, b), c); }
DI float xg_max(float v) {
  auto r = __builtin_amdgcn_permlane32_swap(__float_as_uint(v), __float_as_uint(v), false, false); v = max2f(__uint_as_float(r[0]), __uint_as_float(r[1]));
  auto s = __builtin_amdgcn_permlane16_swap(__float_as_uint(v), __float_as_uint(v), false, false); return max2f(__uint_as_float(s[0]), __uint_as_float(s[1]));
}
DI float xg_sum(float v) {
  auto r = __builtin_amdgcn_permlane32_swap(__float_as_uint(v), __float_as_uint(v), false, false); v = __uint_as_float(r[0]) + __uint_as_float(r[1]);
  auto s = __builtin_amdgcn_permlane16_swap(__float_as_uint(v), __float_as_uint(v), false, false); return __uint_as_float(s[0]) + __uint_as_float(s[1]);
}
#define MFMA16(a, b, c) __builtin_amdgcn_mfma_f32_16x16x32_bf16((a), (b), (c), 0, 0, 0)
#define SB0() __builtin_amdgcn_sched_barrier(0)
#define MEMFENCE() asm volatile("" ::: "memory")
#define BARRIER() do { SB0(); __builtin_amdgcn_s_waitcnt(0xC07F); asm volatile("s_barrier" ::: "memory"); SB0(); } while (0)
#define WAITV(n) asm volatile("s_waitcnt vmcnt(" #n ")" ::: "memory")
DI __amdgpu_buffer_rsrc_t mkrsrc(const void* p, unsigned bytes) { return __builtin_amdgcn_make_buffer_rsrc((void*)p, (short)0, (int)bytes, 0x00020000); }
DI void dma4_nt(__amdgpu_buffer_rsrc_t r, unsigned lds, unsigned va, unsigned vb, unsigned so) {
  asm volatile("s_mov_b32 m0, %0\n\ts_nop 0\n\t"
               "buffer_load_dwordx4 %1, %3, %4 offen nt lds\n\t"
               "buffer_load_dwordx4 %1, %3, %4 offen offset:1024 nt lds\n\t"
               "buffer_load_dwordx4 %2, %3, %4 offen offset:2048 nt lds\n\t"
               "buffer_load_dwordx4 %2, %3, %4 offen offset:3072 nt lds"
               :: "s"(lds), "v"(va), "v"(vb), "s"(r), "s"(so) : "memory");
}
DI void dma4(__amdgpu_buffer_rsrc_t r, unsigned lds, unsigned v0, unsigned v1, unsigned v2, unsigned v3, unsigned so) {
  asm volatile("s_mov_b32 m0, %0\n\ts_nop 0\n\t"
               "buffer_load_dwordx4 %1, %5, %6 offen lds\n\t"
               "buffer_load_dwordx4 %2, %5, %6 offen offset:1024 lds\n\t"
               "buffer_load_dwordx4 %3, %5, %6 offen offset:2048 lds\n\t"
               "buffer_load_dwordx4 %4, %5, %6 offen offset:3072 lds"
               :: "s"(lds), "v"(v0), "v"(v1), "v"(v2), "v"(v3), "s"(r), "s"(so) : "memory");
}
DI void dma3(__amdgpu_buffer_rsrc_t r, unsigned lds, unsigned v0, unsigned v1, unsigned v2, unsigned so) {
  asm volatile("s_mov_b32 m0, %0\n\ts_nop 0\n\t"
               "buffer_load_dwordx4 %1, %4, %5 offen lds\n\t"
               "buffer_load_dwordx4 %2, %4, %5 offen offset:1024 lds\n\t"
               "buffer_load_dwordx4 %3, %4, %5 offen offset:2048 lds"
               :: "s"(lds), "v"(v0), "v"(v1), "v"(v2), "s"(r), "s"(so) : "memory");
}
DI void dma3w(__amdgpu_buffer_rsrc_t r, unsigned lds, unsigned v0, unsigned v1, unsigned v2, unsigned so) {
  asm volatile("s_mov_b32 m0, %0\n\ts_nop 3\n\t"
               "buffer_load_dwordx4 %1, %4, %5 offen lds\n\t"
               "buffer_load_dwordx4 %2, %4, %5 offen offset:1024 lds\n\t"
               "buffer_load_dwordx4 %3, %4, %5 offen offset:2048 lds"
               :: "s"(lds), "v"(v0), "v"(v1), "v"(v2), "s"(r), "s"(so) : "memory");
}
DI void touch(__amdgpu_buffer_rsrc_t r, unsigned lds, unsigned v, unsigned so) {
  asm volatile("s_mov_b32 m0, %0\n\ts_nop 0\n\tbuffer_load_dword %1, %2, %3 offen lds" :: "s"(lds), "v"(v), "s"(r), "s"(so) : "memory");
}

constexpr unsigned KVS = 16384;
constexpr unsigned QB1 = 73728, QB2 = 118784;
constexpr unsigned SCR = 65536;
constexpr unsigned OREG = 65536, WREG = 114688, WOU = 12288;
constexpr unsigned FLG = 2048;
constexpr unsigned PART = 110592;

__global__ __launch_bounds__(512, 2) void attn_main(const char* xA, const char* kA, const char* vA, const char* wqA, const char* woA,
                                                    const char* xB, const char* kB, const char* vB, const char* wqB, const char* woB, char* ows, char* out) {
  extern __shared__ __attribute__((aligned(16))) char lds[];
  const unsigned ldsb = rfl((unsigned)(uintptr_t)(LAS char*)lds);
  const unsigned wid = rfl(threadIdx.x >> 6), lane = threadIdx.x & 63, team = wid >> 2, u = wid & 3, jj = u >> 1, qh = u & 1;
  const unsigned id = blockIdx.x, xcd = id & 7, idx = id >> 3, blk = xcd >> 2;
  const unsigned b = idx & 15, hb = 8 * (xcd & 3) + 4 * (idx >> 4), h = hb + 2 * team + jj;
  const char* kp = blk ? kB : kA; const char* vp = blk ? vB : vA; const char* wop = blk ? woB : woA;
  const __amdgpu_buffer_rsrc_t rk = mkrsrc(kp + (size_t)(b * NH + hb) * SK * DH * 2, 4 * SK * DH * 2);
  const __amdgpu_buffer_rsrc_t rv = mkrsrc(vp + (size_t)(b * NH + hb) * SK * DH * 2, 4 * SK * DH * 2);
  const __amdgpu_buffer_rsrc_t ro = mkrsrc(wop, HD * DM * 2);
  const unsigned grp = blk * NB + b, mem = hb >> 2;
  const __amdgpu_buffer_rsrc_t rf = mkrsrc(ows + (size_t)256 * PART + grp * FLG, FLG);
  const unsigned g = lane >> 4, c16 = lane & 15, q4 = c16 >> 2, p4 = lane & 3;
  const unsigned vka = g * 256 + 16 * (c16 ^ ((8 * qh) | g)), vkb = g * 256 + 16 * (c16 ^ ((8 * qh + 4) | g));
  const unsigned vva = g * 256 + 16 * (c16 ^ (g << 2)), vvb = g * 256 + 16 * (c16 ^ ((g << 2) | 2));
  const unsigned krot = (id * 5 + (id >> 3) + jj * 7) & 15;
  const unsigned kvs0 = jj * (SK * DH * 2) + 4096 * qh, kvd0 = ldsb + 8192 * jj + 4096 * qh;
  auto kv_so = [&](unsigned T) { return kvs0 + ((T & 16) << 14) + (((T + krot) & 15) << 13); };
  auto k_issue = [&](unsigned T) { dma4_nt(rk, kvd0 + 3 * KVS - (T & 1) * (2 * KVS), vka, vkb, kv_so(T)); };
  auto v_issue = [&](unsigned T) { dma4_nt(rv, kvd0 + (T & 1) * (2 * KVS), vva, vvb, kv_so(T)); };
  const unsigned tro = 256 * (8 * g + q4) + 16 * ((q4 << 2) | ((g & 1) << 1) | (p4 >> 1)) + 8 * (p4 & 1);
  const unsigned xo0 = 32768 + (48 * qh + c16) * 128 + 16 * (g ^ ((c16 >> 1) & 7));
  auto rd_tr8 = [&](bf16x8 (&f)[8], unsigned a, unsigned imm) {
#pragma unroll
    for (int d = 0; d < 8; ++d) f[d] = lds_tr2((a ^ (32 * d)) + imm, (a ^ (32 * d)) + imm + 1024);
  };

  bf16x8 qf[4][3];
  f32x4 acc[8][3];
  auto zero_acc = [&]() {
#pragma unroll
    for (int d = 0; d < 8; ++d)
#pragma unroll
      for (int q = 0; q < 3; ++q) acc[d][q] = f32x4{};
  };
  auto mm24 = [&](const int hf, const bf16x8 (&w)[8], bf16x8 (&x)[6], auto&& fill) {
#pragma unroll
    for (int m = 0; m < 24; ++m) {
      const int q = m >> 3, i = (m >> 1) & 3, ks = m & 1;
      acc[4 * hf + i][q] = MFMA16(w[2 * i + ks], x[3 * ks + q], acc[4 * hf + i][q]);
      fill(m);
      SB0();
    }
  };
  auto nofill = [](int) {};
  auto rd_wh = [&](bf16x8 (&w)[8], const int hf, const int r, unsigned a) { const int i = r >> 1; set_half(w[i], r & 1, lds_tr1((a ^ (128 * hf + 32 * (i >> 1))) + 8192 * (i & 1) + 1024 * (r & 1))); };
  auto rd_x1 = [&](const int i, unsigned xo) { return lds_b128((xo ^ (64 * (i / 3))) + 2048 * (i % 3)); };
  auto rd_w = [&](const int hf, bf16x8 (&w)[8], unsigned a) {
#pragma unroll
    for (int i = 0; i < 8; ++i) { const unsigned p = (a ^ (128 * hf + 32 * (i >> 1))) + 8192 * (i & 1); w[i] = lds_tr2(p, p + 1024); }
  };
  auto rd_x = [&](bf16x8 (&x)[6], unsigned xo) {
#pragma unroll
    for (int i = 0; i < 6; ++i) x[i] = lds_b128((xo ^ (64 * (i / 3))) + 2048 * (i % 3));
  };
  auto pack_q = [&]() {
#pragma unroll
    for (int s = 0; s < 4; ++s)
#pragma unroll
      for (int q = 0; q < 3; ++q) qf[s][q] = pack_swap(acc[2 * s][q] * SC_LOG2E, acc[2 * s + 1][q] * SC_LOG2E);
  };

  unsigned long long pc0; unsigned clen;
  asm volatile("attn_code_begin:\n\ts_getpc_b64 %0\n\ts_mov_b32 %1, attn_code_end-attn_code_begin-8" : "=s"(pc0), "=s"(clen));
  if (team == 0) {
    if (u == 1) touch(mkrsrc((const void*)pc0, clen), ldsb + SCR + 256, (idx + 32 * (lane & 15)) * 128, 0);
    if (u == 2) { touch(rk, ldsb + SCR + 512, (lane & 3) * 131072, 0); touch(rv, ldsb + SCR + 512, (lane & 3) * 131072, 0); }
    if (u == 3) touch(ro, ldsb + SCR + 512, 0, hb * DH * (DM * 2));
    if (u == 0) {
      const unsigned old = __builtin_amdgcn_raw_buffer_load_b32(rf, lane < 24 ? 128 * (1 + mem) + 4 * lane : 4 * mem, 0, 16);
      const unsigned base = __builtin_amdgcn_readlane(old, 24) + 1;
      unsigned k = 0;
      while (__any(old - base == k)) ++k;
      if (lane == 0) __builtin_amdgcn_raw_buffer_store_b32(base + k, rf, 4 * mem, 0, 16);
    }
    zero_acc();
    bf16x8 wa[8], wb[8], xa[6];
#pragma unroll
    for (int d = 0; d < 8; ++d) wb[d] = bf16x8{};
#pragma unroll
    for (int q = 0; q < 6; ++q) xa[q] = bf16x8{};
    unsigned bufb = ldsb;
#pragma clang loop unroll(disable)
    for (int c = 0; c < 9; ++c) {
      const unsigned a = bufb + 16384 * jj + tro, xo = bufb + xo0;
      bufb = bufb == ldsb ? ldsb + QB1 : bufb == ldsb + QB1 ? ldsb + QB2 : ldsb;
      BARRIER();
      mm24(1, wb, xa, [&](int m) {
        if (m == 12 || m == 13) xa[3 * (m & 1)] = rd_x1(3 * (m & 1), xo);
        else if (m == 20 || m == 21) xa[3 * (m & 1) + 1] = rd_x1(3 * (m & 1) + 1, xo);
        else if (m < 18) rd_wh(wa, 0, m < 12 ? m : m - 2, a); });
      mm24(0, wa, xa, [&](int m) { if (m == 4 || m == 5) xa[3 * (m & 1) + 2] = rd_x1(3 * (m & 1) + 2, xo); else if (m < 18) rd_wh(wb, 1, m < 4 ? m : m - 2, a); });
    }
    mm24(1, wb, xa, nofill);
    pack_q();
  } else {
    const char* xp = blk ? xB : xA; const char* wp = blk ? wqB : wqA;
    const __amdgpu_buffer_rsrc_t rx = mkrsrc(xp + (size_t)b * SQ * DM * 2, SQ * DM * 2);
    const __amdgpu_buffer_rsrc_t rw = mkrsrc(wp, DM * HD * 2);
    unsigned vw[4], vx[3];
#pragma unroll
    for (int pp = 0; pp < 4; ++pp) vw[pp] = (4 * pp + g) * (HD * 2) - 1024 * pp + 16 * (c16 ^ ((g << 2) | (pp & 2)));
#pragma unroll
    for (int i = 0; i < 3; ++i) vx[i] = (8 * i + (lane >> 3)) * (DM * 2) - 1024 * i + 16 * ((lane & 7) ^ ((4 * (3 * u + i) + g) & 7));
    const unsigned qrot = (b + 4 * (idx >> 4)) % 9;
    auto x_issue = [&](unsigned c, unsigned buf) { dma3(rx, buf + 32768 + 3072 * u, vx[0], vx[1], vx[2], c * 128 + 24 * u * (DM * 2)); };
    auto w_issue = [&](unsigned c, unsigned ks, unsigned buf, unsigned head) {
      dma4(rw, buf + 16384 * jj + 8192 * ks + 4096 * qh, vw[0], vw[1], vw[2], vw[3], (c * 64 + 32 * ks + 16 * qh) * (HD * 2) + head * 256);
    };
    auto nxt = [&](unsigned c) { return c == 8 ? 0u : c + 1; };
    unsigned cq = qrot;
    x_issue(cq, ldsb); w_issue(cq, 0, ldsb, h - 2); w_issue(cq, 1, ldsb, h - 2); cq = nxt(cq);
    x_issue(cq, ldsb + QB1); w_issue(cq, 0, ldsb + QB1, h - 2); w_issue(cq, 1, ldsb + QB1, h - 2); cq = nxt(cq);
    { unsigned c = 2 + (lane >= 24) + (lane >= 48) + qrot; if (c >= 9) c -= 9;
      touch(rx, ldsb + SCR, (24 * u + lane % 24) * (DM * 2) + c * 128, 0);
      c = 4 + qrot; if (c >= 9) c -= 9;
      touch(rx, ldsb + SCR, (24 * u + 16 + (lane & 7)) * (DM * 2) + c * 128, 0); }
    unsigned bufb = ldsb + QB2;
    WAITV(13);
#pragma clang loop unroll(disable)
    for (int c = 0; c < 7; ++c) {
      BARRIER();
      x_issue(cq, bufb); w_issue(cq, 0, bufb, h - 2); w_issue(cq, 1, bufb, h - 2); cq = nxt(cq);
      bufb = bufb == ldsb ? ldsb + QB1 : bufb == ldsb + QB1 ? ldsb + QB2 : ldsb;
      WAITV(11);
    }
    k_issue(0);
    BARRIER();
    v_issue(0);
    WAITV(8); BARRIER();
    k_issue(1); v_issue(1);
    zero_acc();
    WAITV(12); BARRIER();
    x_issue(cq, ldsb + QB1); w_issue(cq, 0, ldsb + QB1, h); w_issue(cq, 1, ldsb + QB1, h); cq = nxt(cq);
    WAITV(19); BARRIER();
    k_issue(2);
    unsigned T = 1;
#pragma clang loop unroll(disable)
    for (int c = 0; c < 9; ++c) {
      const unsigned qb = ldsb + ((c & 1) ? QB2 : QB1), qn = ldsb + ((c & 1) ? QB1 : QB2);
      const bool fast = c & 1;
      bf16x8 wf[2][8], xf[6];
      if (c == 0) WAITV(4); else WAITV(8);
      BARRIER();
      rd_x(xf, qb + xo0);
      rd_w(0, wf[0], qb + 16384 * jj + tro);
      if (!fast) {
        if (c < 8) { x_issue(cq, qn); w_issue(cq, 0, qn, h); }
        v_issue(T + 1);
        if (c < 8) WAITV(15); else WAITV(8);
        BARRIER();
        k_issue(T + 2); ++T;
        mm24(0, wf[0], xf, nofill);
        if (c < 8) WAITV(15); else WAITV(8);
        BARRIER();
      } else { x_issue(cq, qn); w_issue(cq, 0, qn, h); }
      rd_w(1, wf[1], qb + 16384 * jj + tro);
      if (c < 8) w_issue(cq, 1, qn, h);
      cq = nxt(cq);
      v_issue(T + 1);
      if (fast) WAITV(19); else if (c < 8) WAITV(12); else WAITV(8);
      BARRIER();
      k_issue(T + 2); ++T;
      if (fast) mm24(0, wf[0], xf, nofill);
      mm24(1, wf[1], xf, nofill);
    }
    WAITV(8); BARRIER();
    v_issue(16);
    pack_q();
    WAITV(8); BARRIER();
    k_issue(17);
  }

  const unsigned prot = (b >> 3) + (b & 1), ksrot = b & 7;
  unsigned vo[3];
#pragma unroll
  for (int i = 0; i < 3; ++i) { const unsigned L = 64 * i + lane, row = L / 24, ph = L - 24 * row; vo[i] = row * (DM * 2) + 16 * (ph ^ (row & 6)) - 1024 * i; }
  unsigned wo_tab;
  { const unsigned r = lane >= 24, k = lane - 24 * r; unsigned p = ((k >> 3) + prot + 2 * r) % 3;
    wo_tab = ((hb + 2 * r) * DH + 32 * ((k + ksrot) & 7) + 8 * u) * (DM * 2) + 384 * p; }
  auto wo_issue = [&](unsigned i, unsigned buf) { dma3w(ro, buf + 3072 * u, vo[0], vo[1], vo[2], __builtin_amdgcn_readlane(wo_tab, i)); };

  {
    zero_acc();
    f32x4 negm[3] = {f32x4{}, f32x4{}, f32x4{}};
    f32x2 l2[3] = {f32x2{}, f32x2{}, f32x2{}};
    const unsigned koff = ldsb + KVS + 8192 * jj + 256 * (8 * q4 + p4) + 16 * (g ^ c16), voff = ldsb + 8192 * jj + tro;
    f32x4 sc[2][3];
    bf16x8 kf[2][4], vf[8], pf[3];
    auto qk = [&]() {
#pragma unroll
      for (int kb = 0; kb < 2; ++kb)
#pragma unroll
        for (int q = 0; q < 3; ++q) {
          sc[kb][q] = MFMA16(kf[kb][0], qf[0][q], negm[q]);
#pragma unroll
          for (int s = 1; s < 4; ++s) sc[kb][q] = MFMA16(kf[kb][s], qf[s][q], sc[kb][q]);
          SB0();
        }
    };
    WAITV(4);
    BARRIER();
#pragma unroll
    for (int s = 0; s < 4; ++s) { const unsigned a = (2 * KVS + koff) ^ (64 * s); kf[0][s] = lds_b128(a); kf[1][s] = lds_b128(a + 1024); }
    qk();
#pragma clang loop unroll(disable)
    for (int t = 0; t < 16; ++t) {
      const unsigned sb = (t & 1) * (2 * KVS);
      BARRIER();
      rd_tr8(vf, sb + voff, 0);
      MEMFENCE(); SB0();
      float mx[3];
#pragma unroll
      for (int q = 0; q < 3; ++q)
        mx[q] = max3f(max3f(sc[0][q][0], sc[0][q][1], sc[0][q][2]), max3f(sc[0][q][3], sc[1][q][0], sc[1][q][1]), max2f(sc[1][q][2], sc[1][q][3]));
      if (t == 0 || __builtin_expect(__any(!(max3f(mx[0], mx[1], mx[2]) <= THR)), 0)) {
#pragma unroll
        for (int q = 0; q < 3; ++q) {
          float dl = xg_max(mx[q]);
          if (t != 0) dl = max2f(dl, 0.f);
          negm[q] -= dl; sc[0][q] -= dl; sc[1][q] -= dl; mx[q] = dl;
        }
        if (t != 0) {
#pragma unroll
          for (int q = 0; q < 3; ++q) {
            const float al = __builtin_amdgcn_exp2f(-mx[q]);
            l2[q] *= al;
#pragma unroll
            for (int d = 0; d < 8; ++d) acc[d][q] *= al;
          }
        }
      }
#pragma unroll
      for (int q = 0; q < 3; ++q) {
#pragma unroll
        for (int i = 0; i < 4; ++i) { sc[0][q][i] = __builtin_amdgcn_exp2f(sc[0][q][i]); sc[1][q][i] = __builtin_amdgcn_exp2f(sc[1][q][i]); }
        { const f32x4 s4 = sc[0][q] + sc[1][q]; l2[q] += f32x2{s4[0], s4[1]} + f32x2{s4[2], s4[3]}; }
        pf[q] = pack2(sc[0][q], sc[1][q]);
      }
      asm volatile("" : "+v"(pf[0]), "+v"(pf[1]), "+v"(pf[2]), "+v"(l2[0]), "+v"(l2[1]), "+v"(l2[2]));
      WAITV(0); BARRIER();
      if (t == 15 && team == 0) { wo_issue(0, ldsb + WREG); wo_issue(1, ldsb + WREG + WOU); v_issue(17); }
#pragma unroll
      for (int d = 0; d < 8; ++d) {
#pragma unroll
        for (int q = 0; q < 3; ++q) acc[d][q] = MFMA16(vf[d], pf[q], acc[d][q]);
        if (d >= 2 && d < 6) { const int kb = (d - 2) >> 1, s = (d & 1) * 2; kf[kb][s] = lds_b128(((sb + koff) ^ (64 * s)) + 1024 * kb); kf[kb][s + 1] = lds_b128(((sb + koff) ^ (64 * s + 64)) + 1024 * kb); MEMFENCE(); }
        SB0();
      }
      if (t < 15) qk();
    }
    if (team == 0) { BARRIER(); wo_issue(2, ldsb + WREG + 2 * WOU); k_issue(18); }
#pragma unroll
    for (int q = 0; q < 3; ++q) {
      const float inv = __builtin_amdgcn_rcpf(xg_sum(l2[q][0] + l2[q][1]));
#pragma unroll
      for (int s = 0; s < 4; ++s)
        *(LAS bf16x8*)(uintptr_t)(ldsb + OREG + (((4 * jj + s - ksrot) & 7) * 6 + 3 * qh + q) * 1024 + lane * 16) = pack2(acc[2 * s][q] * inv, acc[2 * s + 1][q] * inv);
    }
  }

  const __amdgpu_buffer_rsrc_t rg = mkrsrc(ows + (size_t)grp * 8 * PART, 8 * PART);
  auto await_pass = [&](unsigned pi, unsigned mine) {
#pragma clang loop unroll(disable)
    for (unsigned spin = 0; spin < (1u << 22); ++spin) {
      const unsigned ec = __builtin_amdgcn_raw_buffer_load_b32(rf, 128 * (1 + mem) + 4 * (lane & 7), 32 * pi, 16);
      MEMFENCE();
      if (__all(ec == mine)) break;
      __builtin_amdgcn_s_sleep(1);
    }
  };
  auto sum_pass = [&](auto NS_, unsigned NL, unsigned pi, unsigned r1) {
    constexpr int NS = NS_.value;
    const unsigned p = (pi + prot + 2) % 3;
    if (lane < NL) {
      const unsigned r0 = mem * 288 + r1 + lane;
      u32x4 v[NS][8];
#pragma unroll
      for (int s = 0; s < NS; ++s)
#pragma unroll
        for (int i = 0; i < 8; ++i) v[s][i] = __builtin_amdgcn_raw_buffer_load_b128(rg, (r0 + NL * s) * 16, i * PART + p * 36864, 16);
#pragma unroll
      for (int s = 0; s < NS; ++s) {
        float a[8] = {0.f, 0.f, 0.f, 0.f, 0.f, 0.f, 0.f, 0.f};
#pragma unroll
        for (int i = 0; i < 8; ++i)
#pragma unroll
          for (int k = 0; k < 4; ++k) { a[2 * k] += __uint_as_float(v[s][i][k] << 16); a[2 * k + 1] += __uint_as_float(v[s][i][k] & 0xffff0000u); }
        const unsigned r = r0 + NL * s, ln = r & 63, f = r >> 6, uu = f / 9, i9 = f - 9 * uu, np = i9 / 3, q = i9 - 3 * np;
        const unsigned row = 48 * (uu & 1) + 16 * q + (ln & 15), col = 192 * p + 96 * (uu >> 1) + 32 * np + 4 * (ln >> 4);
        char* op = out + ((size_t)(b * SQ + row) * (2 * DM) + blk * DM + col) * 2;
        *(u32x2*)op = u32x2{cvtpk(a[0], a[1]), cvtpk(a[2], a[3])};
        *(u32x2*)(op + 32) = u32x2{cvtpk(a[4], a[5]), cvtpk(a[6], a[7])};
      }
    }
  };

  if (team == 0) {
    unsigned wof[6];
#pragma unroll
    for (int n = 0; n < 6; ++n) wof[n] = ldsb + (4 * g + q4) * 384 + 16 * ((12 * jj + 2 * n + (p4 >> 1)) ^ ((4 * g + q4) & 6)) + 8 * (p4 & 1);
    const unsigned oof = ldsb + OREG + 3 * qh * 1024 + lane * 16;
    f32x4 oc[6][3];
    unsigned pA[36], pB[36];
#pragma unroll
    for (int i = 0; i < 36; ++i) { pA[i] = 0; pB[i] = 0; }
    unsigned T = 17;
#pragma clang loop unroll(disable)
    for (int p = 0; p < 3; ++p) {
#pragma unroll
      for (int n = 0; n < 6; ++n)
#pragma unroll
        for (int q = 0; q < 3; ++q) oc[n][q] = f32x4{};
#pragma clang loop unroll(disable)
      for (unsigned k = 0; k < 8; k += 2, ++T) {
        WAITV(11); BARRIER();
        bf16x8 of[2][3], wf[2][6];
        const unsigned wb = WREG + (k & 2) * WOU, ob = oof + k * 6144;
#pragma unroll
        for (int i = 0; i < 2; ++i) {
#pragma unroll
          for (int q = 0; q < 3; ++q) of[i][q] = lds_b128(ob + 6144 * i + 1024 * q);
#pragma unroll
          for (int n = 0; n < 6; ++n) wf[i][n] = lds_tr2(wb + wof[n] + WOU * i, wb + wof[n] + WOU * i + 16 * 384);
        }
        wo_issue(2 * T - 31, ldsb + WREG + ((k & 2) ^ 3) * WOU);
        v_issue(T + 1);
        WAITV(14); BARRIER();
        wo_issue(2 * T - 30, ldsb + wb);
        k_issue(T + 2);
#pragma unroll
        for (int n = 0; n < 6; ++n)
#pragma unroll
          for (int q = 0; q < 3; ++q) { oc[n][q] = MFMA16(wf[0][n], of[0][q], oc[n][q]); oc[n][q] = MFMA16(wf[1][n], of[1][q], oc[n][q]); SB0(); }
      }
      if (p == 0) {
#pragma unroll
        for (int i = 0; i < 36; ++i) { const f32x4 v = oc[i / 6][(i / 2) % 3]; pA[i] = cvtpk(v[2 * (i & 1)], v[2 * (i & 1) + 1]); }
      } else if (p == 1) {
#pragma unroll
        for (int i = 0; i < 36; ++i) { const f32x4 v = oc[i / 6][(i / 2) % 3]; pB[i] = cvtpk(v[2 * (i & 1)], v[2 * (i & 1) + 1]); }
      }
    }
    WAITV(14); BARRIER(); wo_issue(27, ldsb + WREG + 3 * WOU); v_issue(30);
    WAITV(14); BARRIER(); k_issue(31);
    WAITV(11); BARRIER(); v_issue(31);
    WAITV(8); BARRIER();
    unsigned woh[6];
#pragma unroll
    for (int n = 0; n < 6; ++n) { woh[n] = wof[n] + WREG; asm volatile("" : "+v"(woh[n])); }
    bf16x8 wa[6], wb[6], oa[6];
    WAITV(4); BARRIER();
    WAITV(0); BARRIER();
    wo_issue(28, ldsb); wo_issue(29, ldsb + WOU);
    BARRIER();
    wo_issue(30, ldsb + 2 * WOU); wo_issue(31, ldsb + 3 * WOU);
    const __amdgpu_buffer_rsrc_t rp = mkrsrc(ows + (size_t)(grp * 8 + mem) * PART, PART);
    unsigned tick = __builtin_amdgcn_raw_buffer_load_b32(rf, 4 * (lane < 2 ? 2 * u + lane : mem), 0, 16);
    auto publish = [&](unsigned pi) { __builtin_amdgcn_raw_buffer_store_b32(tick, rf, lane < 2 ? 128 * (1 + 2 * u + lane) + 4 * mem : FLG, 32 * pi, 16); };
    unsigned pofs[3];
#pragma unroll
    for (int i = 0; i < 3; ++i) pofs[i] = ((i + prot + 2) % 3) * 36864 + u * 9216;
    u32x4 st[3];
    auto fin_tile = [&](const int pi, const int tl, const unsigned (&nx)[36]) {
      const int n = tl / 3, q = tl % 3;
      st[q][2 * (n & 1)] = cvtpk(oc[n][q][0], oc[n][q][1]); st[q][2 * (n & 1) + 1] = cvtpk(oc[n][q][2], oc[n][q][3]);
      if (n & 1) { unsigned so = pofs[pi] + ((n >> 1) * 3 + q) * 1024; asm volatile("" : "+s"(so)); __builtin_amdgcn_raw_buffer_store_b128(st[q], rp, lane * 16 + so, 0, 16); }
      if (pi < 2) oc[n][q] = f32x4{__uint_as_float(nx[2 * tl] << 16), __uint_as_float(nx[2 * tl] & 0xffff0000u), __uint_as_float(nx[2 * tl + 1] << 16), __uint_as_float(nx[2 * tl + 1] & 0xffff0000u)};
    };
    auto rd_w2 = [&](const int j, const int hf, const int i) { const int k = 2 * j + (i & 1), n = 3 * hf + (i >> 1); const unsigned a = (k < 4 ? woh[n] : wof[n]) + (k & 3) * WOU; return lds_tr2(a, a + 16 * 384); };
    auto rd_o2 = [&](const int j, const int i) { return lds_b128(oof + (2 * j + i / 3) * 6144 + 1024 * (i % 3)); };
    auto rd_w2h = [&](bf16x8 (&w)[6], const int j, const int hf, const int r) {
      const int i = r >> 1, k = 2 * j + (i & 1), n = 3 * hf + (i >> 1);
      set_half(w[i], r & 1, lds_tr1((k < 4 ? woh[n] : wof[n]) + (k & 3) * WOU + 16 * 384 * (r & 1)));
    };
    auto mm18 = [&](const int hf, const bf16x8 (&w)[6], bf16x8 (&o)[6], auto&& fill) {
#pragma unroll
      for (int m = 0; m < 18; ++m) {
        const int q = m / 6, i = (m % 6) >> 1, un = m & 1;
        oc[3 * hf + i][q] = MFMA16(w[2 * i + un], o[3 * un + q], oc[3 * hf + i][q]);
        fill(m);
        SB0();
      }
    };
    BARRIER();
#pragma unroll
    for (int i = 0; i < 6; ++i) { oa[i] = rd_o2(0, i); wa[i] = rd_w2(0, 0, i); }
#pragma unroll
    for (int p = 0; p < 3; ++p) {
#pragma unroll
      for (int j = 0; j < 4; ++j) {
        mm18(0, wa, oa, [&](int m) {
          if (m == 3 || m == 4) oa[3 * (m & 1) + 2] = rd_o2(j, 3 * (m & 1) + 2); else if (m < 14) rd_w2h(wb, j, 1, m < 3 ? m : m - 2);
          if (j == 0 && p > 0 && (m & 1)) fin_tile(p - 1, 9 + (m >> 1), p == 1 ? pA : pB); });
        if (j == 2) { WAITV(0); if (p > 0) publish(p - 1); }
        if (j == 1 && p == 0) WAITV(0);
        if (j < 2 || p < 2) BARRIER();
        mm18(1, wb, oa, [&](int m) {
          if (j < 3 || p < 2) {
            if (m == 9 || m == 10) oa[3 * (m & 1)] = rd_o2((j + 1) & 3, 3 * (m & 1));
            else if (m == 15 || m == 16) oa[3 * (m & 1) + 1] = rd_o2((j + 1) & 3, 3 * (m & 1) + 1);
            else if (m < 14) rd_w2h(wa, (j + 1) & 3, 0, m < 9 ? m : m - 2);
          }
          if (j == 3 && (m & 1)) fin_tile(p, m >> 1, p == 0 ? pA : pB); });
        MEMFENCE();
      }
    }
#pragma unroll
    for (int tl = 9; tl < 18; ++tl) fin_tile(2, tl, pB);
    WAITV(0);
    publish(2);
    await_pass(2, __builtin_amdgcn_readlane(tick, 2));
    const unsigned now = __builtin_amdgcn_raw_buffer_load_b32(rf, 4 * (lane < 2 ? 2 * u + lane : mem), 0, 16);
    sum_pass(std::integral_constant<int, 2>{}, 36, 2, 72 * u);
    if (now != tick) { tick = now; publish(0); publish(1); publish(2); }
  } else {
    const unsigned mine = __builtin_amdgcn_raw_buffer_load_b32(rf, 4 * mem, 0, 16);
#pragma clang loop unroll(disable)
    for (unsigned j = 0; j < 11; ++j) {
      if (j < 10) WAITV(12); else WAITV(0);
      BARRIER();
      if (j >= 1 && j < 9) {
        const unsigned k = 2 * j + 6, buf = ldsb + ((k & 4) ? 0 : WREG) + (k & 3) * WOU;
        wo_issue(24 + k, buf); wo_issue(25 + k, buf + WOU);
      }
    }
    await_pass(jj, mine);
    sum_pass(std::integral_constant<int, 3>{}, 48, jj, 144 * qh);
  }
  asm volatile("s_waitcnt vmcnt(0)\nattn_code_end:" ::: "memory");
}

extern "C" void kernel_launch(void* const* d_in, const int* in_sizes, int n_in, void* d_out, int out_size, void* d_ws, size_t ws_size,
                              hipStream_t stream) {
  (void)in_sizes; (void)n_in; (void)out_size; (void)ws_size;
  (void)hipFuncSetAttribute((const void*)attn_main, hipFuncAttributeMaxDynamicSharedMemorySize, LDS_MAIN);
  attn_main<<<256, 512, LDS_MAIN, stream>>>((const char*)d_in[0], (const char*)d_in[1], (const char*)d_in[2], (const char*)d_in[3], (const char*)d_in[4],
                                            (const char*)d_in[5], (const char*)d_in[6], (const char*)d_in[7], (const char*)d_in[8], (const char*)d_in[9], (char*)d_ws, (char*)d_out);
}
```

```cpp
#include <hip/hip_runtime.h>
#include <hip/hip_bf16.h>
#include <stdint.h>
#include <type_traits>

#define LAS __attribute__((address_space(3)))
#define DI __device__ __forceinline__
typedef __bf16 bf16x2 __attribute__((ext_vector_type(2)));
typedef __bf16 bf16x4 __attribute__((ext_vector_type(4)));
typedef __bf16 bf16x8 __attribute__((ext_vector_type(8)));
typedef float f32x2 __attribute__((ext_vector_type(2)));
typedef float f32x4 __attribute__((ext_vector_type(4)));
typedef unsigned u32x4 __attribute__((ext_vector_type(4)));
typedef unsigned u32x2 __attribute__((ext_vector_type(2)));

constexpr int NB = 16, SQ = 96, DM = 576, NH = 32, SK = 512, DH = 128, HD = NH * DH;
constexpr unsigned LDS_MAIN = 163840;
constexpr float SC_LOG2E = 0.08838834764831845f * 1.4426950408889634f;
constexpr float THR = 8.0f;

DI unsigned rfl(unsigned x) { return __builtin_amdgcn_readfirstlane(x); }
DI unsigned cvtpk(float lo, float hi) { f32x2 v = {lo, hi}; bf16x2 b = __builtin_convertvector(v, bf16x2); return __builtin_bit_cast(unsigned, b); }
DI bf16x8 pack2(const f32x4& a, const f32x4& b) {
  u32x4 w = {cvtpk(a[0], a[1]), cvtpk(a[2], a[3]), cvtpk(b[0], b[1]), cvtpk(b[2], b[3])};
  return __builtin_bit_cast(bf16x8, w);
}
DI bf16x8 pack_swap(const f32x4& a0, const f32x4& a1) {
  unsigned p00 = cvtpk(a0[0], a0[1]), p01 = cvtpk(a0[2], a0[3]), p10 = cvtpk(a1[0], a1[1]), p11 = cvtpk(a1[2], a1[3]);
  auto r0 = __builtin_amdgcn_permlane32_swap(p00, p10, false, false);
  auto r1 = __builtin_amdgcn_permlane32_swap(p01, p11, false, false);
  auto t0 = __builtin_amdgcn_permlane16_swap(r0[0], r0[1], false, false);
  auto t1 = __builtin_amdgcn_permlane16_swap(r1[0], r1[1], false, false);
  u32x4 w = {t0[0], t1[0], t0[1], t1[1]};
  return __builtin_bit_cast(bf16x8, w);
}
DI bf16x8 lds_b128(unsigned a) { return *(const LAS bf16x8*)(uintptr_t)a; }
DI bf16x8 lds_tr2(unsigned a0, unsigned a1) {
  bf16x4 lo = __builtin_amdgcn_ds_read_tr16_b64_v4bf16((LAS bf16x4*)(uintptr_t)a0);
  bf16x4 hi = __builtin_amdgcn_ds_read_tr16_b64_v4bf16((LAS bf16x4*)(uintptr_t)a1);
  return __builtin_shufflevector(lo, hi, 0, 1, 2, 3, 4, 5, 6, 7);
}
DI bf16x4 lds_tr1(unsigned a) { return __builtin_amdgcn_ds_read_tr16_b64_v4bf16((LAS bf16x4*)(uintptr_t)a); }
DI void set_half(bf16x8& f, const int hi, bf16x4 v) { f = hi ? __builtin_shufflevector(f, __builtin_shufflevector(v, v, 0, 1, 2, 3, 0, 1, 2, 3), 0, 1, 2, 3, 12, 13, 14, 15) : __builtin_shufflevector(v, v, 0, 1, 2, 3, -1, -1, -1, -1); }
DI float max2f(float a, float b) { return __builtin_elementwise_maximum(a, b); }
DI float max3f(float a, float b, float c) { return max2f(max2f(a, b), c); }
DI float xg_max(float v) {
  auto r = __builtin_amdgcn_permlane32_swap(__float_as_uint(v), __float_as_uint(v), false, false); v = max2f(__uint_as_float(r[0]), __uint_as_float(r[1]));
  auto s = __builtin_amdgcn_permlane16_swap(__float_as_uint(v), __float_as_uint(v), false, false); return max2f(__uint_as_float(s[0]), __uint_as_float(s[1]));
}
DI float xg_sum(float v) {
  auto r = __builtin_amdgcn_permlane32_swap(__float_as_uint(v), __float_as_uint(v), false, false); v = __uint_as_float(r[0]) + __uint_as_float(r[1]);
  auto s = __builtin_amdgcn_permlane16_swap(__float_as_uint(v), __float_as_uint(v), false, false); return __uint_as_float(s[0]) + __uint_as_float(s[1]);
}
#define MFMA16(a, b, c) __builtin_amdgcn_mfma_f32_16x16x32_bf16((a), (b), (c), 0, 0, 0)
#define SB0() __builtin_amdgcn_sched_barrier(0)
#define MEMFENCE() asm volatile("" ::: "memory")
#define BARRIER() do { SB0(); __builtin_amdgcn_s_waitcnt(0xC07F); asm volatile("s_barrier" ::: "memory"); SB0(); } while (0)
#define WAITV(n) asm volatile("s_waitcnt vmcnt(" #n ")" ::: "memory")
DI __amdgpu_buffer_rsrc_t mkrsrc(const void* p, unsigned bytes) { return __builtin_amdgcn_make_buffer_rsrc((void*)p, (short)0, (int)bytes, 0x00020000); }
DI void dma4_nt(__amdgpu_buffer_rsrc_t r, unsigned lds, unsigned va, unsigned vb, unsigned so) {
  asm volatile("s_mov_b32 m0, %0\n\ts_nop 0\n\t"
               "buffer_load_dwordx4 %1, %3, %4 offen nt lds\n\t"
               "buffer_load_dwordx4 %1, %3, %4 offen offset:1024 nt lds\n\t"
               "buffer_load_dwordx4 %2, %3, %4 offen offset:2048 nt lds\n\t"
               "buffer_load_dwordx4 %2, %3, %4 offen offset:3072 nt lds"
               :: "s"(lds), "v"(va), "v"(vb), "s"(r), "s"(so) : "memory");
}
DI void dma4(__amdgpu_buffer_rsrc_t r, unsigned lds, unsigned v0, unsigned v1, unsigned v2, unsigned v3, unsigned so) {
  asm volatile("s_mov_b32 m0, %0\n\ts_nop 0\n\t"
               "buffer_load_dwordx4 %1, %5, %6 offen lds\n\t"
               "buffer_load_dwordx4 %2, %5, %6 offen offset:1024 lds\n\t"
               "buffer_load_dwordx4 %3, %5, %6 offen offset:2048 lds\n\t"
               "buffer_load_dwordx4 %4, %5, %6 offen offset:3072 lds"
               :: "s"(lds), "v"(v0), "v"(v1), "v"(v2), "v"(v3), "s"(r), "s"(so) : "memory");
}
DI void dma3(__amdgpu_buffer_rsrc_t r, unsigned lds, unsigned v0, unsigned v1, unsigned v2, unsigned so) {
  asm volatile("s_mov_b32 m0, %0\n\ts_nop 0\n\t"
               "buffer_load_dwordx4 %1, %4, %5 offen lds\n\t"
               "buffer_load_dwordx4 %2, %4, %5 offen offset:1024 lds\n\t"
               "buffer_load_dwordx4 %3, %4, %5 offen offset:2048 lds"
               :: "s"(lds), "v"(v0), "v"(v1), "v"(v2), "s"(r), "s"(so) : "memory");
}
DI void dma3w(__amdgpu_buffer_rsrc_t r, unsigned lds, unsigned v0, unsigned v1, unsigned v2, unsigned so) {
  asm volatile("s_mov_b32 m0, %0\n\ts_nop 3\n\t"
               "buffer_load_dwordx4 %1, %4, %5 offen lds\n\t"
               "buffer_load_dwordx4 %2, %4, %5 offen offset:1024 lds\n\t"
               "buffer_load_dwordx4 %3, %4, %5 offen offset:2048 lds"
               :: "s"(lds), "v"(v0), "v"(v1), "v"(v2), "s"(r), "s"(so) : "memory");
}
DI void touch(__amdgpu_buffer_rsrc_t r, unsigned lds, unsigned v, unsigned so) {
  asm volatile("s_mov_b32 m0, %0\n\ts_nop 0\n\tbuffer_load_dword %1, %2, %3 offen lds" :: "s"(lds), "v"(v), "s"(r), "s"(so) : "memory");
}

constexpr unsigned KVS = 16384;
constexpr unsigned QB1 = 73728, QB2 = 118784;
constexpr unsigned SCR = 65536;
constexpr unsigned OREG = 65536, WREG = 114688, WOU = 12288;
constexpr unsigned FLG = 2048;
constexpr unsigned PART = 110592;

__global__ __launch_bounds__(512, 2) void attn_main(const char* xA, const char* kA, const char* vA, const char* wqA, const char* woA,
                                                    const char* xB, const char* kB, const char* vB, const char* wqB, const char* woB, char* ows, char* out) {
  extern __shared__ __attribute__((aligned(16))) char lds[];
  const unsigned ldsb = rfl((unsigned)(uintptr_t)(LAS char*)lds);
  const unsigned wid = rfl(threadIdx.x >> 6), lane = threadIdx.x & 63, team = wid >> 2, u = wid & 3, jj = u >> 1, qh = u & 1;
  const unsigned id = blockIdx.x, xcd = id & 7, idx = id >> 3, blk = xcd >> 2;
  const unsigned b = idx & 15, hb = 8 * (xcd & 3) + 4 * (idx >> 4), h = hb + 2 * team + jj;
  const char* kp = blk ? kB : kA; const char* vp = blk ? vB : vA; const char* wop = blk ? woB : woA;
  const __amdgpu_buffer_rsrc_t rk = mkrsrc(kp + (size_t)(b * NH + hb) * SK * DH * 2, 4 * SK * DH * 2);
  const __amdgpu_buffer_rsrc_t rv = mkrsrc(vp + (size_t)(b * NH + hb) * SK * DH * 2, 4 * SK * DH * 2);
  const __amdgpu_buffer_rsrc_t ro = mkrsrc(wop, HD * DM * 2);
  const unsigned grp = blk * NB + b, mem = hb >> 2;
  const __amdgpu_buffer_rsrc_t rf = mkrsrc(ows + (size_t)256 * PART + grp * FLG, FLG);
  const unsigned g = lane >> 4, c16 = lane & 15, q4 = c16 >> 2, p4 = lane & 3;
  const unsigned vka = g * 256 + 16 * (c16 ^ ((8 * qh) | g)), vkb = g * 256 + 16 * (c16 ^ ((8 * qh + 4) | g));
  const unsigned vva = g * 256 + 16 * (c16 ^ (g << 2)), vvb = g * 256 + 16 * (c16 ^ ((g << 2) | 2));
  const unsigned krot = (id * 5 + (id >> 3) + jj * 7) & 15;
  const unsigned kvs0 = jj * (SK * DH * 2) + 4096 * qh, kvd0 = ldsb + 8192 * jj + 4096 * qh;
  auto kv_so = [&](unsigned T) { return kvs0 + ((T & 16) << 14) + (((T + krot) & 15) << 13); };
  auto k_issue = [&](unsigned T) { dma4_nt(rk, kvd0 + 3 * KVS - (T & 1) * (2 * KVS), vka, vkb, kv_so(T)); };
  auto v_issue = [&](unsigned T) { dma4_nt(rv, kvd0 + (T & 1) * (2 * KVS), vva, vvb, kv_so(T)); };
  const unsigned tro = 256 * (8 * g + q4) + 16 * ((q4 << 2) | ((g & 1) << 1) | (p4 >> 1)) + 8 * (p4 & 1);
  const unsigned xo0 = 32768 + (48 * qh + c16) * 128 + 16 * (g ^ ((c16 >> 1) & 7));
  auto rd_tr8 = [&](bf16x8 (&f)[8], unsigned a, unsigned imm) {
#pragma unroll
    for (int d = 0; d < 8; ++d) f[d] = lds_tr2((a ^ (32 * d)) + imm, (a ^ (32 * d)) + imm + 1024);
  };

  bf16x8 qf[4][3];
  f32x4 acc[8][3];
  auto zero_acc = [&]() {
#pragma unroll
    for (int d = 0; d < 8; ++d)
#pragma unroll
      for (int q = 0; q < 3; ++q) acc[d][q] = f32x4{};
  };
  auto mm24 = [&](const int hf, const bf16x8 (&w)[8], bf16x8 (&x)[6], auto&& fill) {
#pragma unroll
    for (int m = 0; m < 24; ++m) {
      const int q = m >> 3, i = (m >> 1) & 3, ks = m & 1;
      acc[4 * hf + i][q] = MFMA16(w[2 * i + ks], x[3 * ks + q], acc[4 * hf + i][q]);
      fill(m);
      SB0();
    }
  };
  auto nofill = [](int) {};
  auto rd_wh = [&](bf16x8 (&w)[8], const int hf, const int r, unsigned a) { const int i = r >> 1; set_half(w[i], r & 1, lds_tr1((a ^ (128 * hf + 32 * (i >> 1))) + 8192 * (i & 1) + 1024 * (r & 1))); };
  auto rd_x1 = [&](const int i, unsigned xo) { return lds_b128((xo ^ (64 * (i / 3))) + 2048 * (i % 3)); };
  auto rd_w = [&](const int hf, bf16x8 (&w)[8], unsigned a) {
#pragma unroll
    for (int i = 0; i < 8; ++i) { const unsigned p = (a ^ (128 * hf + 32 * (i >> 1))) + 8192 * (i & 1); w[i] = lds_tr2(p, p + 1024); }
  };
  auto rd_x = [&](bf16x8 (&x)[6], unsigned xo) {
#pragma unroll
    for (int i = 0; i < 6; ++i) x[i] = lds_b128((xo ^ (64 * (i / 3))) + 2048 * (i % 3));
  };
  auto pack_q = [&]() {
#pragma unroll
    for (int s = 0; s < 4; ++s)
#pragma unroll
      for (int q = 0; q < 3; ++q) qf[s][q] = pack_swap(acc[2 * s][q] * SC_LOG2E, acc[2 * s + 1][q] * SC_LOG2E);
  };

  unsigned long long pc0; unsigned clen;
  asm volatile("attn_code_begin:\n\ts_getpc_b64 %0\n\ts_mov_b32 %1, attn_code_end-attn_code_begin-8" : "=s"(pc0), "=s"(clen));
  if (team == 0) {
    if (u == 1) touch(mkrsrc((const void*)pc0, clen), ldsb + SCR + 256, (idx + 32 * (lane & 15)) * 128, 0);
    if (u == 0) {
      const unsigned old = __builtin_amdgcn_raw_buffer_load_b32(rf, lane < 24 ? 128 * (1 + mem) + 4 * lane : 4 * mem, 0, 16);
      const unsigned base = __builtin_amdgcn_readlane(old, 24) + 1;
      unsigned k = 0;
      while (__any(old - base == k)) ++k;
      if (lane == 0) __builtin_amdgcn_raw_buffer_store_b32(base + k, rf, 4 * mem, 0, 16);
    }
    zero_acc();
    bf16x8 wa[8], wb[8], xa[6];
#pragma unroll
    for (int d = 0; d < 8; ++d) wb[d] = bf16x8{};
#pragma unroll
    for (int q = 0; q < 6; ++q) xa[q] = bf16x8{};
    unsigned bufb = ldsb;
#pragma clang loop unroll(disable)
    for (int c = 0; c < 9; ++c) {
      const unsigned a = bufb + 16384 * jj + tro, xo = bufb + xo0;
      bufb = bufb == ldsb ? ldsb + QB1 : bufb == ldsb + QB1 ? ldsb + QB2 : ldsb;
      BARRIER();
      mm24(1, wb, xa, [&](int m) {
        if (m == 12 || m == 13) xa[3 * (m & 1)] = rd_x1(3 * (m & 1), xo);
        else if (m == 20 || m == 21) xa[3 * (m & 1) + 1] = rd_x1(3 * (m & 1) + 1, xo);
        else if (m < 18) rd_wh(wa, 0, m < 12 ? m : m - 2, a); });
      mm24(0, wa, xa, [&](int m) { if (m == 4 || m == 5) xa[3 * (m & 1) + 2] = rd_x1(3 * (m & 1) + 2, xo); else if (m < 18) rd_wh(wb, 1, m < 4 ? m : m - 2, a); });
    }
    mm24(1, wb, xa, nofill);
    pack_q();
  } else {
    const char* xp = blk ? xB : xA; const char* wp = blk ? wqB : wqA;
    const __amdgpu_buffer_rsrc_t rx = mkrsrc(xp + (size_t)b * SQ * DM * 2, SQ * DM * 2);
    const __amdgpu_buffer_rsrc_t rw = mkrsrc(wp, DM * HD * 2);
    unsigned vw[4], vx[3];
#pragma unroll
    for (int pp = 0; pp < 4; ++pp) vw[pp] = (4 * pp + g) * (HD * 2) - 1024 * pp + 16 * (c16 ^ ((g << 2) | (pp & 2)));
#pragma unroll
    for (int i = 0; i < 3; ++i) vx[i] = (8 * i + (lane >> 3)) * (DM * 2) - 1024 * i + 16 * ((lane & 7) ^ ((4 * (3 * u + i) + g) & 7));
    const unsigned qrot = (b + 4 * (idx >> 4)) % 9;
    auto x_issue = [&](unsigned c, unsigned buf) { dma3(rx, buf + 32768 + 3072 * u, vx[0], vx[1], vx[2], c * 128 + 24 * u * (DM * 2)); };
    auto w_issue = [&](unsigned c, unsigned ks, unsigned buf, unsigned head) {
      dma4(rw, buf + 16384 * jj + 8192 * ks + 4096 * qh, vw[0], vw[1], vw[2], vw[3], (c * 64 + 32 * ks + 16 * qh) * (HD * 2) + head * 256);
    };
    auto nxt = [&](unsigned c) { return c == 8 ? 0u : c + 1; };
    unsigned cq = qrot;
    x_issue(cq, ldsb); w_issue(cq, 0, ldsb, h - 2); w_issue(cq, 1, ldsb, h - 2); cq = nxt(cq);
    x_issue(cq, ldsb + QB1); w_issue(cq, 0, ldsb + QB1, h - 2); w_issue(cq, 1, ldsb + QB1, h - 2); cq = nxt(cq);
    unsigned bufb = ldsb + QB2;
    WAITV(11);
#pragma clang loop unroll(disable)
    for (int c = 0; c < 7; ++c) {
      BARRIER();
      x_issue(cq, bufb); w_issue(cq, 0, bufb, h - 2); w_issue(cq, 1, bufb, h - 2); cq = nxt(cq);
      bufb = bufb == ldsb ? ldsb + QB1 : bufb == ldsb + QB1 ? ldsb + QB2 : ldsb;
      WAITV(11);
    }
    k_issue(0);
    BARRIER();
    v_issue(0);
    WAITV(8); BARRIER();
    k_issue(1); v_issue(1);
    zero_acc();
    WAITV(12); BARRIER();
    x_issue(cq, ldsb + QB1); w_issue(cq, 0, ldsb + QB1, h); w_issue(cq, 1, ldsb + QB1, h); cq = nxt(cq);
    WAITV(19); BARRIER();
    k_issue(2);
    unsigned T = 1;
#pragma clang loop unroll(disable)
    for (int c = 0; c < 9; ++c) {
      const unsigned qb = ldsb + ((c & 1) ? QB2 : QB1), qn = ldsb + ((c & 1) ? QB1 : QB2);
      const bool fast = c & 1;
      bf16x8 wf[2][8], xf[6];
      if (c == 0) WAITV(4); else WAITV(8);
      BARRIER();
      rd_x(xf, qb + xo0);
      rd_w(0, wf[0], qb + 16384 * jj + tro);
      if (!fast) {
        if (c < 8) { x_issue(cq, qn); w_issue(cq, 0, qn, h); }
        v_issue(T + 1);
        if (c < 8) WAITV(15); else WAITV(8);
        BARRIER();
        k_issue(T + 2); ++T;
        mm24(0, wf[0], xf, nofill);
        if (c < 8) WAITV(15); else WAITV(8);
        BARRIER();
      } else { x_issue(cq, qn); w_issue(cq, 0, qn, h); }
      rd_w(1, wf[1], qb + 16384 * jj + tro);
      if (c < 8) w_issue(cq, 1, qn, h);
      cq = nxt(cq);
      v_issue(T + 1);
      if (fast) WAITV(19); else if (c < 8) WAITV(12); else WAITV(8);
      BARRIER();
      k_issue(T + 2); ++T;
      if (fast) mm24(0, wf[0], xf, nofill);
      mm24(1, wf[1], xf, nofill);
    }
    WAITV(8); BARRIER();
    v_issue(16);
    pack_q();
    WAITV(8); BARRIER();
    k_issue(17);
  }

  const unsigned prot = (b >> 3) + (b & 1), ksrot = b & 7;
  unsigned vo[3];
#pragma unroll
  for (int i = 0; i < 3; ++i) { const unsigned L = 64 * i + lane, row = L / 24, ph = L - 24 * row; vo[i] = row * (DM * 2) + 16 * (ph ^ (row & 6)) - 1024 * i; }
  unsigned wo_tab;
  { const unsigned r = lane >= 24, k = lane - 24 * r; unsigned p = ((k >> 3) + prot + 2 * r) % 3;
    wo_tab = ((hb + 2 * r) * DH + 32 * ((k + ksrot) & 7) + 8 * u) * (DM * 2) + 384 * p; }
  auto wo_issue = [&](unsigned i, unsigned buf) { dma3w(ro, buf + 3072 * u, vo[0], vo[1], vo[2], __builtin_amdgcn_readlane(wo_tab, i)); };

  {
    zero_acc();
    f32x4 negm[3] = {f32x4{}, f32x4{}, f32x4{}};
    f32x2 l2[3] = {f32x2{}, f32x2{}, f32x2{}};
    const unsigned koff = ldsb + KVS + 8192 * jj + 256 * (8 * q4 + p4) + 16 * (g ^ c16), voff = ldsb + 8192 * jj + tro;
    f32x4 sc[2][3];
    bf16x8 kf[2][4], vf[8], pf[3];
    float gmx;
    auto qk = [&]() {
#pragma unroll
      for (int c = 0; c < 6; ++c) {
        const int kb = c / 3, q = c % 3;
        sc[kb][q] = MFMA16(kf[kb][0], qf[0][q], negm[q]);
#pragma unroll
        for (int s = 1; s < 4; ++s) sc[kb][q] = MFMA16(kf[kb][s], qf[s][q], sc[kb][q]);
        if (c >= 2) { const f32x4 v = sc[(c - 2) / 3][(c - 2) % 3]; gmx = c == 2 ? max2f(max3f(v[0], v[1], v[2]), v[3]) : max3f(max3f(gmx, v[0], v[1]), v[2], v[3]); }
        SB0();
      }
      asm volatile("" : "+v"(gmx));
    };
    float thr = -__builtin_inff();
    WAITV(4);
    BARRIER();
#pragma unroll
    for (int s = 0; s < 4; ++s) { const unsigned a = (2 * KVS + koff) ^ (64 * s); kf[0][s] = lds_b128(a); kf[1][s] = lds_b128(a + 1024); }
    qk();
#pragma clang loop unroll(disable)
    for (int t = 0; t < 16; ++t) {
      const unsigned sb = (t & 1) * (2 * KVS);
      BARRIER();
      rd_tr8(vf, sb + voff, 0);
      MEMFENCE(); SB0();
      gmx = max3f(max3f(gmx, sc[1][1][0], sc[1][1][1]), max3f(sc[1][1][2], sc[1][1][3], sc[1][2][0]), max3f(sc[1][2][1], sc[1][2][2], sc[1][2][3]));
      if (__builtin_expect(__any(!(gmx <= thr)), 0)) {
        float mx[3];
        thr = THR;
#pragma unroll
        for (int q = 0; q < 3; ++q) {
          mx[q] = max3f(max3f(sc[0][q][0], sc[0][q][1], sc[0][q][2]), max3f(sc[0][q][3], sc[1][q][0], sc[1][q][1]), max2f(sc[1][q][2], sc[1][q][3]));
          float dl = xg_max(mx[q]);
          if (t != 0) dl = max2f(dl, 0.f);
          negm[q] -= dl; sc[0][q] -= dl; sc[1][q] -= dl; mx[q] = dl;
        }
        if (t != 0) {
#pragma unroll
          for (int q = 0; q < 3; ++q) {
            const float al = __builtin_amdgcn_exp2f(-mx[q]);
            l2[q] *= al;
#pragma unroll
            for (int d = 0; d < 8; ++d) acc[d][q] *= al;
          }
        }
      }
#pragma unroll
      for (int q = 0; q < 3; ++q) {
#pragma unroll
        for (int i = 0; i < 4; ++i) { sc[0][q][i] = __builtin_amdgcn_exp2f(sc[0][q][i]); sc[1][q][i] = __builtin_amdgcn_exp2f(sc[1][q][i]); }
        { const f32x4 s4 = sc[0][q] + sc[1][q]; l2[q] += f32x2{s4[0], s4[1]} + f32x2{s4[2], s4[3]}; }
        pf[q] = pack2(sc[0][q], sc[1][q]);
      }
      asm volatile("" : "+v"(pf[0]), "+v"(pf[1]), "+v"(pf[2]), "+v"(l2[0]), "+v"(l2[1]), "+v"(l2[2]));
      WAITV(0); BARRIER();
      if (t == 15 && team == 0) { wo_issue(0, ldsb + WREG); wo_issue(1, ldsb + WREG + WOU); v_issue(17); }
#pragma unroll
      for (int d = 0; d < 8; ++d) {
#pragma unroll
        for (int q = 0; q < 3; ++q) acc[d][q] = MFMA16(vf[d], pf[q], acc[d][q]);
        if (d >= 2 && d < 6) { const int kb = (d - 2) >> 1, s = (d & 1) * 2; kf[kb][s] = lds_b128(((sb + koff) ^ (64 * s)) + 1024 * kb); kf[kb][s + 1] = lds_b128(((sb + koff) ^ (64 * s + 64)) + 1024 * kb); MEMFENCE(); }
        SB0();
      }
      if (t < 15) qk();
    }
    if (team == 0) { BARRIER(); wo_issue(2, ldsb + WREG + 2 * WOU); k_issue(18); }
#pragma unroll
    for (int q = 0; q < 3; ++q) {
      const float inv = __builtin_amdgcn_rcpf(xg_sum(l2[q][0] + l2[q][1]));
#pragma unroll
      for (int s = 0; s < 4; ++s)
        *(LAS bf16x8*)(uintptr_t)(ldsb + OREG + (((4 * jj + s - ksrot) & 7) * 6 + 3 * qh + q) * 1024 + lane * 16) = pack2(acc[2 * s][q] * inv, acc[2 * s + 1][q] * inv);
    }
  }

  const __amdgpu_buffer_rsrc_t rg = mkrsrc(ows + (size_t)grp * 8 * PART, 8 * PART);
  auto await_pass = [&](unsigned pi, unsigned mine) {
#pragma clang loop unroll(disable)
    for (unsigned spin = 0; spin < (1u << 22); ++spin) {
      const unsigned ec = __builtin_amdgcn_raw_buffer_load_b32(rf, 128 * (1 + mem) + 4 * (lane & 7), 32 * pi, 16);
      MEMFENCE();
      if (__all(ec == mine)) break;
      __builtin_amdgcn_s_sleep(1);
    }
  };
  auto sum_pass = [&](auto NS_, unsigned NL, unsigned pi, unsigned r1) {
    constexpr int NS = NS_.value;
    const unsigned p = (pi + prot + 2) % 3;
    if (lane < NL) {
      const unsigned r0 = mem * 288 + r1 + lane;
      u32x4 v[NS][8];
#pragma unroll
      for (int s = 0; s < NS; ++s)
#pragma unroll
        for (int i = 0; i < 8; ++i) v[s][i] = __builtin_amdgcn_raw_buffer_load_b128(rg, (r0 + NL * s) * 16, i * PART + p * 36864, 16);
#pragma unroll
      for (int s = 0; s < NS; ++s) {
        float a[8] = {0.f, 0.f, 0.f, 0.f, 0.f, 0.f, 0.f, 0.f};
#pragma unroll
        for (int i = 0; i < 8; ++i)
#pragma unroll
          for (int k = 0; k < 4; ++k) { a[2 * k] += __uint_as_float(v[s][i][k] << 16); a[2 * k + 1] += __uint_as_float(v[s][i][k] & 0xffff0000u); }
        const unsigned r = r0 + NL * s, ln = r & 63, f = r >> 6, uu = f / 9, i9 = f - 9 * uu, np = i9 / 3, q = i9 - 3 * np;
        const unsigned row = 48 * (uu & 1) + 16 * q + (ln & 15), col = 192 * p + 96 * (uu >> 1) + 32 * np + 4 * (ln >> 4);
        char* op = out + ((size_t)(b * SQ + row) * (2 * DM) + blk * DM + col) * 2;
        *(u32x2*)op = u32x2{cvtpk(a[0], a[1]), cvtpk(a[2], a[3])};
        *(u32x2*)(op + 32) = u32x2{cvtpk(a[4], a[5]), cvtpk(a[6], a[7])};
      }
    }
  };

  if (team == 0) {
    unsigned wof[6];
#pragma unroll
    for (int n = 0; n < 6; ++n) wof[n] = ldsb + (4 * g + q4) * 384 + 16 * ((12 * jj + 2 * n + (p4 >> 1)) ^ ((4 * g + q4) & 6)) + 8 * (p4 & 1);
    const unsigned oof = ldsb + OREG + 3 * qh * 1024 + lane * 16;
    f32x4 oc[6][3];
    unsigned pA[36], pB[36];
#pragma unroll
    for (int i = 0; i < 36; ++i) { pA[i] = 0; pB[i] = 0; }
    unsigned T = 17;
#pragma clang loop unroll(disable)
    for (int p = 0; p < 3; ++p) {
#pragma unroll
      for (int n = 0; n < 6; ++n)
#pragma unroll
        for (int q = 0; q < 3; ++q) oc[n][q] = f32x4{};
#pragma clang loop unroll(disable)
      for (unsigned k = 0; k < 8; k += 2, ++T) {
        WAITV(11); BARRIER();
        bf16x8 of[2][3], wf[2][6];
        const unsigned wb = WREG + (k & 2) * WOU, ob = oof + k * 6144;
#pragma unroll
        for (int i = 0; i < 2; ++i) {
#pragma unroll
          for (int q = 0; q < 3; ++q) of[i][q] = lds_b128(ob + 6144 * i + 1024 * q);
#pragma unroll
          for (int n = 0; n < 6; ++n) wf[i][n] = lds_tr2(wb + wof[n] + WOU * i, wb + wof[n] + WOU * i + 16 * 384);
        }
        wo_issue(2 * T - 31, ldsb + WREG + ((k & 2) ^ 3) * WOU);
        v_issue(T + 1);
        WAITV(14); BARRIER();
        wo_issue(2 * T - 30, ldsb + wb);
        k_issue(T + 2);
#pragma unroll
        for (int n = 0; n < 6; ++n)
#pragma unroll
          for (int q = 0; q < 3; ++q) { oc[n][q] = MFMA16(wf[0][n], of[0][q], oc[n][q]); oc[n][q] = MFMA16(wf[1][n], of[1][q], oc[n][q]); SB0(); }
      }
      if (p == 0) {
#pragma unroll
        for (int i = 0; i < 36; ++i) { const f32x4 v = oc[i / 6][(i / 2) % 3]; pA[i] = cvtpk(v[2 * (i & 1)], v[2 * (i & 1) + 1]); }
      } else if (p == 1) {
#pragma unroll
        for (int i = 0; i < 36; ++i) { const f32x4 v = oc[i / 6][(i / 2) % 3]; pB[i] = cvtpk(v[2 * (i & 1)], v[2 * (i & 1) + 1]); }
      }
    }
    WAITV(14); BARRIER(); wo_issue(27, ldsb + WREG + 3 * WOU); v_issue(30);
    WAITV(14); BARRIER(); k_issue(31);
    WAITV(11); BARRIER(); v_issue(31);
    WAITV(8); BARRIER();
    unsigned woh[6];
#pragma unroll
    for (int n = 0; n < 6; ++n) { woh[n] = wof[n] + WREG; asm volatile("" : "+v"(woh[n])); }
    bf16x8 wa[6], wb[6], oa[6];
    WAITV(4); BARRIER();
    WAITV(0); BARRIER();
    wo_issue(28, ldsb); wo_issue(29, ldsb + WOU);
    BARRIER();
    wo_issue(30, ldsb + 2 * WOU); wo_issue(31, ldsb + 3 * WOU);
    const __amdgpu_buffer_rsrc_t rp = mkrsrc(ows + (size_t)(grp * 8 + mem) * PART, PART);
    unsigned tick = __builtin_amdgcn_raw_buffer_load_b32(rf, 4 * (lane < 2 ? 2 * u + lane : mem), 0, 16);
    auto publish = [&](unsigned pi) { __builtin_amdgcn_raw_buffer_store_b32(tick, rf, lane < 2 ? 128 * (1 + 2 * u + lane) + 4 * mem : FLG, 32 * pi, 16); };
    unsigned pofs[3];
#pragma unroll
    for (int i = 0; i < 3; ++i) pofs[i] = ((i + prot + 2) % 3) * 36864 + u * 9216;
    u32x4 st[3];
    auto fin_tile = [&](const int pi, const int tl, const unsigned (&nx)[36]) {
      const int n = tl / 3, q = tl % 3;
      st[q][2 * (n & 1)] = cvtpk(oc[n][q][0], oc[n][q][1]); st[q][2 * (n & 1) + 1] = cvtpk(oc[n][q][2], oc[n][q][3]);
      if (n & 1) { unsigned so = pofs[pi] + ((n >> 1) * 3 + q) * 1024; asm volatile("" : "+s"(so)); __builtin_amdgcn_raw_buffer_store_b128(st[q], rp, lane * 16 + so, 0, 16); }
      if (pi < 2) oc[n][q] = f32x4{__uint_as_float(nx[2 * tl] << 16), __uint_as_float(nx[2 * tl] & 0xffff0000u), __uint_as_float(nx[2 * tl + 1] << 16), __uint_as_float(nx[2 * tl + 1] & 0xffff0000u)};
    };
    auto rd_w2 = [&](const int j, const int hf, const int i) { const int k = 2 * j + (i & 1), n = 3 * hf + (i >> 1); const unsigned a = (k < 4 ? woh[n] : wof[n]) + (k & 3) * WOU; return lds_tr2(a, a + 16 * 384); };
    auto rd_o2 = [&](const int j, const int i) { return lds_b128(oof + (2 * j + i / 3) * 6144 + 1024 * (i % 3)); };
    auto rd_w2h = [&](bf16x8 (&w)[6], const int j, const int hf, const int r) {
      const int i = r >> 1, k = 2 * j + (i & 1), n = 3 * hf + (i >> 1);
      set_half(w[i], r & 1, lds_tr1((k < 4 ? woh[n] : wof[n]) + (k & 3) * WOU + 16 * 384 * (r & 1)));
    };
    auto mm18 = [&](const int hf, const bf16x8 (&w)[6], bf16x8 (&o)[6], auto&& fill) {
#pragma unroll
      for (int m = 0; m < 18; ++m) {
        const int q = m / 6, i = (m % 6) >> 1, un = m & 1;
        oc[3 * hf + i][q] = MFMA16(w[2 * i + un], o[3 * un + q], oc[3 * hf + i][q]);
        fill(m);
        SB0();
      }
    };
    BARRIER();
#pragma unroll
    for (int i = 0; i < 6; ++i) { oa[i] = rd_o2(0, i); wa[i] = rd_w2(0, 0, i); }
#pragma unroll
    for (int p = 0; p < 3; ++p) {
#pragma unroll
      for (int j = 0; j < 4; ++j) {
        mm18(0, wa, oa, [&](int m) {
          if (m == 3 || m == 4) oa[3 * (m & 1) + 2] = rd_o2(j, 3 * (m & 1) + 2); else if (m < 14) rd_w2h(wb, j, 1, m < 3 ? m : m - 2);
          if (j == 0 && p > 0 && (m & 1)) fin_tile(p - 1, 9 + (m >> 1), p == 1 ? pA : pB); });
        if (j == 2) { WAITV(0); if (p > 0) publish(p - 1); }
        if (j == 1 && p == 0) WAITV(0);
        if (j < 2 || p < 2) BARRIER();
        mm18(1, wb, oa, [&](int m) {
          if (j < 3 || p < 2) {
            if (m == 9 || m == 10) oa[3 * (m & 1)] = rd_o2((j + 1) & 3, 3 * (m & 1));
            else if (m == 15 || m == 16) oa[3 * (m & 1) + 1] = rd_o2((j + 1) & 3, 3 * (m & 1) + 1);
            else if (m < 14) rd_w2h(wa, (j + 1) & 3, 0, m < 9 ? m : m - 2);
          }
          if (j == 3 && (m & 1)) fin_tile(p, m >> 1, p == 0 ? pA : pB); });
        MEMFENCE();
      }
    }
#pragma unroll
    for (int tl = 9; tl < 18; ++tl) fin_tile(2, tl, pB);
    WAITV(0);
    publish(2);
    await_pass(2, __builtin_amdgcn_readlane(tick, 2));
    const unsigned now = __builtin_amdgcn_raw_buffer_load_b32(rf, 4 * (lane < 2 ? 2 * u + lane : mem), 0, 16);
    sum_pass(std::integral_constant<int, 2>{}, 36, 2, 72 * u);
    if (now != tick) { tick = now; publish(0); publish(1); publish(2); }
  } else {
    const unsigned mine = __builtin_amdgcn_raw_buffer_load_b32(rf, 4 * mem, 0, 16);
#pragma clang loop unroll(disable)
    for (unsigned j = 0; j < 11; ++j) {
      if (j < 10) WAITV(12); else WAITV(0);
      BARRIER();
      if (j >= 1 && j < 9) {
        const unsigned k = 2 * j + 6, buf = ldsb + ((k & 4) ? 0 : WREG) + (k & 3) * WOU;
        wo_issue(24 + k, buf); wo_issue(25 + k, buf + WOU);
      }
    }
    await_pass(jj, mine);
    sum_pass(std::integral_constant<int, 3>{}, 48, jj, 144 * qh);
  }
  asm volatile("s_waitcnt vmcnt(0)\nattn_code_end:" ::: "memory");
}

extern "C" void kernel_launch(void* const* d_in, const int* in_sizes, int n_in, void* d_out, int out_size, void* d_ws, size_t ws_size,
                              hipStream_t stream) {
  (void)in_sizes; (void)n_in; (void)out_size; (void)ws_size;
  (void)hipFuncSetAttribute((const void*)attn_main, hipFuncAttributeMaxDynamicSharedMemorySize, LDS_MAIN);
  attn_main<<<256, 512, LDS_MAIN, stream>>>((const char*)d_in[0], (const char*)d_in[1], (const char*)d_in[2], (const char*)d_in[3], (const char*)d_in[4],
                                            (const char*)d_in[5], (const char*)d_in[6], (const char*)d_in[7], (const char*)d_in[8], (const char*)d_in[9], (char*)d_ws, (char*)d_out);
}
```
